```python
import torch

def _softmax(x, axis=-1):
    return torch.softmax(x, dim=axis)
def _gelu(x):
    return torch.nn.functional.gelu(x, approximate="tanh")
def _silu(x):
    return torch.nn.functional.silu(x)
def _sigmoid(x):
    return torch.sigmoid(x)
def _layernorm(x, axis=-1, eps=1e-5):
    m = x.mean(dim=axis, keepdim=True)
    v = x.var(dim=axis, keepdim=True, unbiased=False)
    return (x - m) / torch.sqrt(v + eps)
def _scatter_add(tgt, idx, src, axis=0):
    return tgt.clone().index_add_(axis, idx, src)
def _segment_sum(src, seg, n):
    out = torch.zeros((n,) + src.shape[1:], dtype=src.dtype)
    return out.index_add_(0, seg, src)
def _pad(x, pw):
    flat = tuple(v for pair in reversed(pw) for v in pair)
    return torch.nn.functional.pad(x, flat)

kernel_name = 'gated_mix__grouped_kv_mqa__moe_arch_mod__prompt'

def setup_inputs():

    _w = (lambda a: (lambda t: t.to(torch.bfloat16) if t.is_floating_point() else t)(torch.as_tensor(a)))
    def _A_setup():
        B, S, D, H = 1, 2048, 640, 32
        Dh = D // H
        return {
            "x": _w((lambda *s: torch.randn(*s, dtype=torch.bfloat16) * 0.05)(B, S, D)),
            "wq": _w((lambda *s: torch.randn(*s, dtype=torch.bfloat16) * 0.8)(D, H * Dh)),
            "wk": _w((lambda *s: torch.randn(*s, dtype=torch.bfloat16) * 0.8)(D, Dh)),
            "wv": _w((lambda *s: torch.randn(*s, dtype=torch.bfloat16) * 0.2)(D, Dh)),
            "wo": _w((lambda *s: torch.randn(*s, dtype=torch.bfloat16) * 0.2)(H * Dh, D)),
        }
    _A_in = _A_setup()

    def _B_setup():
        B, S, D, F, C = 1, 2048, 640, 1536, 1
        return {
            "x": _w((lambda *s: torch.randn(*s, dtype=torch.bfloat16) * 0.05)(B, S, D)),
            "w_r": _w((lambda *s: torch.randn(*s, dtype=torch.bfloat16) * 1.6)(D)),
            "w_gate": _w((lambda *s: torch.randn(*s, dtype=torch.bfloat16) * 0.2)(D, F)),
            "w_up": _w((lambda *s: torch.randn(*s, dtype=torch.bfloat16) * 0.2)(D, F)),
            "w_down": _w((lambda *s: torch.randn(*s, dtype=torch.bfloat16) * 0.2)(F, D)),
        }
    _B_in = _B_setup()

    return {
        "A_x": _A_in["x"],
        "A_wq": _A_in["wq"],
        "A_wk": _A_in["wk"],
        "A_wv": _A_in["wv"],
        "A_wo": _A_in["wo"],
        "B_x": _B_in["x"],
        "B_w_r": _B_in["w_r"],
        "B_w_gate": _B_in["w_gate"],
        "B_w_up": _B_in["w_up"],
        "B_w_down": _B_in["w_down"],
        "gate_w": _w((lambda *s: torch.randn(*s, dtype=torch.bfloat16) * 0.05)(640) * 16.0),
    }

def reference(A_x, A_wq, A_wk, A_wv, A_wo, B_x, B_w_r, B_w_gate, B_w_up, B_w_down, gate_w):
    def _A_ref(x, wq, wk, wv, wo):
        B, S, D = x.shape
        H = 32
        Dh = D // H
        q = (lambda x, *d: x.permute(*d))((x @ wq).reshape(B, S, H, Dh), 0, 2, 1, 3)
        k = x @ wk
        v = x @ wv
        scores = torch.einsum("bhqd,bkd->bhqk", q, k) * (Dh ** -0.5)
        p = _softmax(scores, axis=-1)
        o = torch.einsum("bhqk,bkd->bhqd", p, v)
        return (lambda x, *d: x.permute(*d))(o, 0, 2, 1, 3).reshape(B, S, H * Dh) @ wo

    def _B_ref(x, w_r, w_gate, w_up, w_down):
        B, S, D = x.shape
        C = 1
        r = x @ w_r
        idx = torch.argsort(-r, axis=-1)[:, :C]
        idx_e = (lambda x, n, axis: torch.repeat_interleave(x, n, dim=axis))(idx.reshape(B, C, 1), D, axis=2)
        x_sel = (lambda x, i, axis: torch.take_along_dim(x, i, dim=axis))(x, idx_e, axis=1)
        g = _silu(x_sel @ w_gate)
        u = x_sel @ w_up
        y = (g * u) @ w_down
        r_sel = _sigmoid((lambda x, i, axis: torch.take_along_dim(x, i, dim=axis))(r, idx, axis=1))
        y = y * r_sel.reshape(B, C, 1)
        b_off = (torch.arange(B) * S).reshape(B, 1)
        flat_idx = (idx + b_off).reshape(B * C)
        zero = (x * 0.0).reshape(B * S, D)
        upd = _scatter_add(zero, flat_idx, y.reshape(B * C, D), axis=0)
        return x + upd.reshape(B, S, D)

    x = A_x
    g = _sigmoid((x * gate_w).sum(-1, keepdims=True))
    a = _A_ref(x, A_wq, A_wk, A_wv, A_wo)
    b = _B_ref(x, B_w_r, B_w_gate, B_w_up, B_w_down)
    return g * a + (1 - g) * b
```

```cpp
#include <hip/hip_runtime.h>
#include <hip/hip_bf16.h>
#include <stdint.h>

typedef __attribute__((ext_vector_type(8))) short bf16x8;
typedef __attribute__((ext_vector_type(4))) short bf16x4;
typedef __attribute__((ext_vector_type(4))) float f32x4;
typedef __attribute__((ext_vector_type(2))) float f32x2;
typedef __attribute__((ext_vector_type(16))) float f32x16;
typedef __attribute__((ext_vector_type(4))) unsigned u32x4;
typedef __attribute__((ext_vector_type(2))) unsigned u32x2;
typedef __attribute__((ext_vector_type(8))) __bf16 bf8v;
typedef __attribute__((ext_vector_type(2))) __bf16 bf2v;
typedef unsigned short u16;
#define LAS __attribute__((address_space(3)))
#define DI __device__ __forceinline__

constexpr int S = 2048, D = 640, H = 32, DH = 20, F = 1536;
constexpr int KV_TILE = 2624;
constexpr int KV_K2 = 1024, KV_V = 1280, KV_VS = 672, KV_VH = 336;
constexpr int CHUNK = 16 * KV_TILE;
constexpr size_t WS_Q = 0;
constexpr size_t WS_O = WS_Q + (size_t)S * D * 2;
constexpr size_t WS_KV = WS_O + (size_t)S * D * 2;
constexpr size_t WS_G = WS_KV + 64 * KV_TILE + 1024;
constexpr size_t WS_CAND = WS_G + S * 4;
constexpr size_t WS_YP = WS_CAND + 1024;
constexpr float QSCALE = 0.22360679774997896f * 1.4426950408889634f;

DI float bf2f(u16 b) { return __uint_as_float((unsigned)b << 16); }
DI float bflo(unsigned u) { return __uint_as_float(u << 16); }
DI float bfhi(unsigned u) { return __uint_as_float(u & 0xffff0000u); }
DI unsigned pk2(float lo, float hi) { bf2v v; v[0] = (__bf16)lo; v[1] = (__bf16)hi; return __builtin_bit_cast(unsigned, v); }
DI float rbf(float x) { return (float)(__bf16)x; }
DI f32x4 mfma16(bf16x8 a, bf16x8 b, f32x4 c) { return __builtin_amdgcn_mfma_f32_16x16x32_bf16(__builtin_bit_cast(bf8v, a), __builtin_bit_cast(bf8v, b), c, 0, 0, 0); }
DI f32x16 mfma32(bf16x8 a, bf16x8 b, f32x16 c) { return __builtin_amdgcn_mfma_f32_32x32x16_bf16(__builtin_bit_cast(bf8v, a), __builtin_bit_cast(bf8v, b), c, 0, 0, 0); }
DI f32x16 mfma32k8(bf16x4 a, bf16x4 b, f32x16 c) { return __builtin_amdgcn_mfma_f32_32x32x8bf16_1k(a, b, c, 0, 0, 0); }
DI bf16x4 trread(LAS const char* p) { return __builtin_amdgcn_ds_read_tr16_b64_v4i16((LAS bf16x4*)p); }
DI float sigmoidf(float x) { return 1.0f / (1.0f + __expf(-x)); }

constexpr int WROW = 192, WBLK = 8 * WROW + 32, WKS = 4 * WBLK;
constexpr int GEMM_LDS = 80 * WBLK;
DI int woff(int k) { return k * WROW + (k >> 3) * 32; }

DI void stage_w80(const u16* __restrict__ W, int c0, char* lds, int tid) {
  u32x4 v[25];
#pragma unroll
  for (int i = 0; i < 25; ++i) { const int u = tid + 256 * i, k = u / 10, c = u - k * 10; v[i] = *(const u32x4*)(W + k * D + c0 + c * 8); }
#pragma unroll
  for (int i = 0; i < 25; ++i) { const int u = tid + 256 * i, k = u / 10, c = u - k * 10; *(u32x4*)(lds + woff(k) + c * 16) = v[i]; }
}
DI bf16x8 wfrag(LAS const char* base, int ks, int t) {
  bf16x4 lo = trread(base + ks * WKS + t * 32), hi = trread(base + ks * WKS + t * 32 + 4 * WROW);
  return __builtin_shufflevector(lo, hi, 0, 1, 2, 3, 4, 5, 6, 7);
}

__global__ __launch_bounds__(256) void k_proj(const u16* __restrict__ x, const u16* __restrict__ wq, const u16* __restrict__ wk,
                                              const u16* __restrict__ wv, const u16* __restrict__ wr, const u16* __restrict__ gw,
                                              char* __restrict__ ws) {
  extern __shared__ __attribute__((aligned(16))) char lds[];
  const int b = blockIdx.x, rt = (b & 7) * 4 + ((b >> 3) & 3), cg = b >> 5;
  const int tid = threadIdx.x, lane = tid & 63, w = tid >> 6, c = lane & 15, g = lane >> 4;
  const int tok = rt * 64 + w * 16 + c;
  bf16x8 xf[20];
#pragma unroll
  for (int ks = 0; ks < 20; ++ks) xf[ks] = *(const bf16x8*)(x + tok * D + ks * 32 + g * 8);
  stage_w80(wq, cg * 80, lds, tid);
  if (cg < 4) {
    const u16* we = (cg < 2) ? wk : wv;
#pragma unroll
    for (int i = 0; i < 10; ++i) {
      const int u = tid + 256 * i, k = u >> 2, p = u & 3;
      u32x2 v = {0u, 0u};
      if (!(cg & 1)) v = *(const u32x2*)(we + k * DH + p * 4);
      else if (p == 0) v = *(const u32x2*)(we + k * DH + 16);
      else if (p == 1 && cg == 1) v[0] = (unsigned)wr[k] | ((unsigned)gw[k] << 16);
      *(u32x2*)(lds + woff(k) + 160 + p * 8) = v;
    }
  }
  __syncthreads();
  LAS const char* wb = (LAS const char*)lds + woff(8 * g + (c >> 2)) + (c & 3) * 8;
  f32x4 acc[6];
#pragma unroll
  for (int t = 0; t < 6; ++t) acc[t] = (f32x4){0.f, 0.f, 0.f, 0.f};
#pragma unroll
  for (int ks = 0; ks < 20; ++ks) {
#pragma unroll
    for (int t = 0; t < 5; ++t) acc[t] = mfma16(wfrag(wb, ks, t), xf[ks], acc[t]);
  }
  if (cg < 2) {
#pragma unroll
    for (int ks = 0; ks < 20; ++ks) acc[5] = mfma16(wfrag(wb, ks, 5), xf[ks], acc[5]);
  } else if (cg < 4) {
#pragma unroll
    for (int ks = 0; ks < 20; ++ks) acc[5] = mfma16(xf[ks], wfrag(wb, ks, 5), acc[5]);
  }
  u16* Q = (u16*)(ws + WS_Q);
#pragma unroll
  for (int t = 0; t < 5; ++t) {
    u32x2 o = {pk2(acc[t][0] * QSCALE, acc[t][1] * QSCALE), pk2(acc[t][2] * QSCALE, acc[t][3] * QSCALE)};
    *(u32x2*)(Q + tok * D + cg * 80 + t * 16 + g * 4) = o;
  }
  if (cg < 4) {
    char* kv = ws + WS_KV + (size_t)(rt * 2 + (w >> 1)) * KV_TILE;
    const u32x2 o = {pk2(acc[5][0], acc[5][1]), pk2(acc[5][2], acc[5][3])};
    const int r = (w & 1) * 16 + c;
    if (cg == 0) {
      *(u32x2*)(kv + ((g >> 1) * 32 + r) * 16 + (g & 1) * 8) = o;
    } else if (cg == 1) {
      if (g == 0) *(u32x2*)(kv + KV_K2 + r * 8) = o;
      const unsigned rb = o[0] & 0xffffu;
      unsigned key = (rb & 0x8000u) ? (~rb & 0xffffu) : (rb | 0x8000u);
      if ((rb & 0x7fffu) > 0x7f80u) key = 0;
      key = (key << 16) | (unsigned)(2047 - tok);
#pragma unroll
      for (int m = 1; m < 16; m <<= 1) { const unsigned o2 = __shfl_xor(key, m); key = key > o2 ? key : o2; }
      if (g == 1) {
        ((float*)(ws + WS_G))[tok] = rbf(sigmoidf(rbf(acc[5][1])));
        if (c == 0) ((unsigned*)(ws + WS_CAND))[rt * 4 + w] = key;
      }
    } else {
      char* vs = kv + KV_V + (w & 1) * KV_VS + (g & 1) * KV_VH + (g >> 1) * 8;
      if (cg == 2) *(u32x2*)(vs + c * 16) = o;
      else if (c < 4) *(u32x2*)(vs + (16 + c) * 16) = o;
      else if (c == 4) *(u32x2*)(vs + 20 * 16) = (u32x2){0x3f803f80u, 0x3f803f80u};
    }
  }
}

DI unsigned wave_max_u32(unsigned v) {
#pragma unroll
  for (int m = 1; m < 64; m <<= 1) { const unsigned o = __shfl_xor(v, m); v = v > o ? v : o; }
  return v;
}
DI void glds16(const void* gsrc, unsigned lds_dst) {
  unsigned keep;
  asm volatile("s_mov_b32 %0, m0\n\ts_mov_b32 m0, %2\n\ts_nop 0\n\tglobal_load_lds_dwordx4 %1, off\n\ts_mov_b32 m0, %0" : "=&s"(keep) : "v"(gsrc), "s"(lds_dst) : "memory");
}
constexpr int ATT_LDS = 3 * CHUNK + 1024;


DI void pack16(const f32x16& p, bf16x8& a, bf16x8& b) {
  u32x4 x = {pk2(p[0], p[1]), pk2(p[2], p[3]), pk2(p[4], p[5]), pk2(p[6], p[7])};
  u32x4 y = {pk2(p[8], p[9]), pk2(p[10], p[11]), pk2(p[12], p[13]), pk2(p[14], p[15])};
  a = __builtin_bit_cast(bf16x8, x); b = __builtin_bit_cast(bf16x8, y);
}
DI float tile_max(const f32x16& s) {
  float m = fmaxf(fmaxf(s[0], s[1]), s[2]);
#pragma unroll
  for (int i = 3; i < 15; i += 2) m = fmaxf(fmaxf(m, s[i]), s[i + 1]);
  m = fmaxf(m, s[15]);
  return m;
}

__global__ __launch_bounds__(512, 2) void k_attn(const u16* __restrict__ x, const u16* __restrict__ wg, const u16* __restrict__ wu,
                                                 const u16* __restrict__ wd, char* __restrict__ ws) {
  extern __shared__ __attribute__((aligned(16))) char lds[];
  const int b = blockIdx.x, qt = (b & 7) * 4 + ((b >> 3) & 3), hg = b >> 5;
  const int tid = threadIdx.x, lane = tid & 63, w = __builtin_amdgcn_readfirstlane(tid >> 6), r = lane & 31, h = lane >> 5;
  const char* kvg = ws + WS_KV;
  const unsigned ldsb = (unsigned)(size_t)lds;
#pragma unroll
  for (int ch = 0; ch < 3; ++ch) {
#pragma unroll
    for (int i = 0; i < 6; ++i) { const int p = w + 8 * i; if (p < 41) glds16(kvg + ch * CHUNK + p * 1024 + lane * 16, ldsb + ch * CHUNK + p * 1024); }
  }
  const int head = hg * 4 + (w & 3), q = qt * 64 + (w >> 2) * 32 + r;
  const u16* Qp = (const u16*)(ws + WS_Q) + q * D + head * DH;
  bf16x8 q1; bf16x4 q2 = {0, 0, 0, 0};
  { const u32x2 a = *(const u32x2*)(Qp + 8 * h), c2 = *(const u32x2*)(Qp + 8 * h + 4); q1 = __builtin_bit_cast(bf16x8, (u32x4){a[0], a[1], c2[0], c2[1]}); }
  { const u32x2 a = *(const u32x2*)(Qp + 16); if (h == 0) q2 = __builtin_bit_cast(bf16x4, a); }
  asm volatile("s_waitcnt vmcnt(0)\n\ts_barrier" ::: "memory");

  LAS const char* kb = (LAS const char*)lds + lane * 16;
  LAS const char* k2b = (LAS const char*)lds + KV_K2 + r * 8;
  LAS const char* vb = (LAS const char*)lds + KV_V + h * KV_VH + r * 16;
  f32x16 zero;
#pragma unroll
  for (int i = 0; i < 16; ++i) zero[i] = 0.f;
  float m0;
  { f32x16 s = mfma32(*(LAS const bf16x8*)kb, q1, zero); s = mfma32k8(*(LAS const bf16x4*)k2b, q2, s);
    m0 = tile_max(s); m0 = fmaxf(m0, __shfl_xor(m0, 32)); }
  f32x16 nm;
#pragma unroll
  for (int i = 0; i < 16; ++i) nm[i] = -m0;
  f32x16 o = zero;
  for (int ch = 0; ch < 4; ++ch) {
    const int slot = (ch == 3 ? 0 : ch) * CHUNK;
    if (ch == 1) {
      asm volatile("s_waitcnt lgkmcnt(0)\n\ts_barrier" ::: "memory");
#pragma unroll
      for (int i = 0; i < 6; ++i) { const int p = w + 8 * i; if (p < 41) glds16(kvg + 3 * CHUNK + p * 1024 + lane * 16, ldsb + p * 1024); }
    }
    if (ch == 3) asm volatile("s_waitcnt vmcnt(0)\n\ts_barrier" ::: "memory");
#pragma unroll 4
    for (int t = 0; t < 16; ++t) {
      const int off = slot + t * KV_TILE;
      f32x16 s = mfma32(*(LAS const bf16x8*)(kb + off), q1, nm);
      s = mfma32k8(*(LAS const bf16x4*)(k2b + off), q2, s);
#pragma unroll
      for (int i = 0; i < 16; ++i) s[i] = __builtin_amdgcn_exp2f(s[i]);
      bf16x8 p0, p1; pack16(s, p0, p1);
      o = mfma32(*(LAS const bf16x8*)(vb + off), p0, o);
      o = mfma32(*(LAS const bf16x8*)(vb + off + KV_VS), p1, o);
    }
  }
  float l = __shfl(o[8], r + 32);
  {
    float am = 0.f;
#pragma unroll
    for (int i = 0; i < 12; ++i) if (i < 8 || h == 0) am = fmaxf(am, fabsf(o[i]));
    const bool ok = (l >= 0x1p-60f) && (l <= 0x1p60f) && (am <= 0x1p126f) ;
    if (!__all(ok)) {
      const char* gk = kvg + lane * 16; const char* gk2 = kvg + KV_K2 + r * 8; const char* gv = kvg + KV_V + h * KV_VH + r * 16;
      float m = -INFINITY;
      for (int t = 0; t < 64; ++t) {
        f32x16 s = mfma32(*(const bf16x8*)(gk + t * KV_TILE), q1, zero); s = mfma32k8(*(const bf16x4*)(gk2 + t * KV_TILE), q2, s);
        m = fmaxf(m, tile_max(s));
      }
      m = fmaxf(m, __shfl_xor(m, 32));
#pragma unroll
      for (int i = 0; i < 16; ++i) nm[i] = -m;
      o = zero;
      for (int t = 0; t < 64; ++t) {
        f32x16 s = mfma32(*(const bf16x8*)(gk + t * KV_TILE), q1, nm); s = mfma32k8(*(const bf16x4*)(gk2 + t * KV_TILE), q2, s);
#pragma unroll
        for (int i = 0; i < 16; ++i) s[i] = __builtin_amdgcn_exp2f(s[i]);
        bf16x8 p0, p1; pack16(s, p0, p1);
        o = mfma32(*(const bf16x8*)(gv + t * KV_TILE), p0, o);
        o = mfma32(*(const bf16x8*)(gv + t * KV_TILE + KV_VS), p1, o);
      }
      l = __shfl(o[8], r + 32);
    }
  }
  const float rl = 1.0f / l;
  u16* Op = (u16*)(ws + WS_O) + q * D + head * DH + 4 * h;
  *(u32x2*)(Op) = (u32x2){pk2(o[0] * rl, o[1] * rl), pk2(o[2] * rl, o[3] * rl)};
  *(u32x2*)(Op + 8) = (u32x2){pk2(o[4] * rl, o[5] * rl), pk2(o[6] * rl, o[7] * rl)};
  if (h == 0) *(u32x2*)(Op + 16) = (u32x2){pk2(o[8] * rl, o[9] * rl), pk2(o[10] * rl, o[11] * rl)};

  if (b < 192) {
    const int i3 = b >> 3, f0 = ((b & 7) * 3 + (i3 >> 3)) * 64 + (i3 & 7) * 8;
    float* red = (float*)(lds + 3 * CHUNK);
    if (w < 4) {
      const unsigned* cand = (const unsigned*)(ws + WS_CAND);
      unsigned key = cand[lane], k2 = cand[lane + 64]; key = wave_max_u32(key > k2 ? key : k2);
      const int sel = 2047 - (int)(key & 0xffffu);
      const u16* wm = h ? wu : wg;
      float acc[8];
#pragma unroll
      for (int j = 0; j < 8; ++j) acc[j] = 0.f;
#pragma unroll
      for (int i = 0; i < 5; ++i) {
        const int k = w * 160 + i * 32 + r;
        const u32x4 v = *(const u32x4*)(wm + k * F + f0);
        const float xv = bf2f(x[sel * D + k]);
#pragma unroll
        for (int j = 0; j < 4; ++j) { acc[2 * j] += xv * bflo(v[j]); acc[2 * j + 1] += xv * bfhi(v[j]); }
      }
#pragma unroll
      for (int j = 0; j < 8; ++j) {
#pragma unroll
        for (int m = 1; m < 32; m <<= 1) acc[j] += __shfl_xor(acc[j], m);
      }
      if (r == 0) {
#pragma unroll
        for (int j = 0; j < 8; ++j) red[w * 16 + h * 8 + j] = acc[j];
      }
    }
    __syncthreads();
    if (tid < 320) {
      const int j = lane & 7;
      const float gs = rbf(red[j] + red[16 + j] + red[32 + j] + red[48 + j]);
      const float us = rbf(red[8 + j] + red[24 + j] + red[40 + j] + red[56 + j]);
      const float hv = rbf(rbf(gs * sigmoidf(gs)) * us);
      float y0 = 0.f, y1 = 0.f;
#pragma unroll
      for (int f = 0; f < 8; ++f) {
        const unsigned v = *(const unsigned*)(wd + (f0 + f) * D + tid * 2);
        const float hf = __shfl(hv, f);
        y0 += hf * bflo(v); y1 += hf * bfhi(v);
      }
      *(f32x2*)((float*)(ws + WS_YP) + b * D + tid * 2) = (f32x2){y0, y1};
    }
  }
}

__global__ __launch_bounds__(256) void k_out(const u16* __restrict__ x, const u16* __restrict__ wo, const char* __restrict__ ws, u16* __restrict__ out) {
  extern __shared__ __attribute__((aligned(16))) char lds[];
  const int b = blockIdx.x, rt = (b & 7) * 4 + ((b >> 3) & 3), cg = b >> 5;
  const int tid = threadIdx.x, lane = tid & 63, w = tid >> 6, c = lane & 15, g = lane >> 4;
  const int tok = rt * 64 + w * 16 + c;
  const u16* O = (const u16*)(ws + WS_O);
  bf16x8 xf[20];
#pragma unroll
  for (int ks = 0; ks < 20; ++ks) xf[ks] = *(const bf16x8*)(O + tok * D + ks * 32 + g * 8);
  const unsigned* cand = (const unsigned*)(ws + WS_CAND);
  unsigned key = cand[lane], k2 = cand[lane + 64];
  stage_w80(wo, cg * 80, lds, tid);
  key = wave_max_u32(key > k2 ? key : k2);
  const int sel = 2047 - (int)(key & 0xffffu);
  float* ysum = (float*)(lds + GEMM_LDS);
  const bool owner = (sel >> 6) == rt;
  if (owner && tid < 240) {
    const int cu = tid % 20, pr = tid / 20;
    f32x4 s = {0.f, 0.f, 0.f, 0.f};
#pragma unroll
    for (int i = 0; i < 16; ++i) s += *(const f32x4*)((const float*)(ws + WS_YP) + (pr * 16 + i) * D + cg * 80 + cu * 4);
    *(f32x4*)(ysum + pr * 80 + cu * 4) = s;
  }
  const float gt = ((const float*)(ws + WS_G))[tok];
  u32x2 xr[5];
#pragma unroll
  for (int t = 0; t < 5; ++t) xr[t] = *(const u32x2*)(x + tok * D + cg * 80 + t * 16 + g * 4);
  __syncthreads();
  LAS const char* wb = (LAS const char*)lds + woff(8 * g + (c >> 2)) + (c & 3) * 8;
  f32x4 acc[5];
#pragma unroll
  for (int t = 0; t < 5; ++t) acc[t] = (f32x4){0.f, 0.f, 0.f, 0.f};
#pragma unroll
  for (int ks = 0; ks < 20; ++ks) {
#pragma unroll
    for (int t = 0; t < 5; ++t) acc[t] = mfma16(wfrag(wb, ks, t), xf[ks], acc[t]);
  }
  const float og = rbf(1.0f - gt);
  const unsigned kb = key >> 16;
  const float rsel = rbf(sigmoidf(bf2f((u16)((kb & 0x8000u) ? (kb & 0x7fffu) : (~kb & 0xffffu)))));
#pragma unroll
  for (int t = 0; t < 5; ++t) {
    float bv[4] = {bflo(xr[t][0]), bfhi(xr[t][0]), bflo(xr[t][1]), bfhi(xr[t][1])};
    if (tok == sel) {
#pragma unroll
      for (int j = 0; j < 4; ++j) {
        float y = 0.f;
        for (int p = 0; p < 12; ++p) y += ysum[p * 80 + t * 16 + g * 4 + j];
        bv[j] = rbf(bv[j] + rbf(rbf(y) * rsel));
      }
    }
    float ov[4];
#pragma unroll
    for (int j = 0; j < 4; ++j) ov[j] = rbf(gt * rbf(acc[t][j])) + rbf(og * bv[j]);
    *(u32x2*)(out + tok * D + cg * 80 + t * 16 + g * 4) = (u32x2){pk2(ov[0], ov[1]), pk2(ov[2], ov[3])};
  }
}

extern "C" void kernel_launch(void* const* d_in, const int* in_sizes, int n_in, void* d_out, int out_size, void* d_ws, size_t ws_size,
                              hipStream_t stream) {
  const u16* x = (const u16*)d_in[0];
  const u16 *wq = (const u16*)d_in[1], *wk = (const u16*)d_in[2], *wv = (const u16*)d_in[3], *wo = (const u16*)d_in[4];
  const u16 *wr = (const u16*)d_in[6], *wg = (const u16*)d_in[7], *wu = (const u16*)d_in[8], *wd = (const u16*)d_in[9], *gw = (const u16*)d_in[10];
  char* ws = (char*)d_ws;
  hipFuncSetAttribute((const void*)k_proj, hipFuncAttributeMaxDynamicSharedMemorySize, GEMM_LDS);
  hipFuncSetAttribute((const void*)k_attn, hipFuncAttributeMaxDynamicSharedMemorySize, ATT_LDS);
  hipFuncSetAttribute((const void*)k_out, hipFuncAttributeMaxDynamicSharedMemorySize, GEMM_LDS + 4096);
  k_proj<<<256, 256, GEMM_LDS, stream>>>(x, wq, wk, wv, wr, gw, ws);
  k_attn<<<256, 512, ATT_LDS, stream>>>(x, wg, wu, wd, ws);
  k_out<<<256, 256, GEMM_LDS + 4096, stream>>>(x, wo, ws, (u16*)d_out);
}
```

```cpp
#include <hip/hip_runtime.h>
#include <hip/hip_bf16.h>
#include <stdint.h>

typedef __attribute__((ext_vector_type(8))) short bf16x8;
typedef __attribute__((ext_vector_type(4))) short bf16x4;
typedef __attribute__((ext_vector_type(4))) float f32x4;
typedef __attribute__((ext_vector_type(2))) float f32x2;
typedef __attribute__((ext_vector_type(16))) float f32x16;
typedef __attribute__((ext_vector_type(8))) float f32x8;
typedef __attribute__((ext_vector_type(16))) __bf16 bf16v;
typedef __attribute__((ext_vector_type(4))) unsigned u32x4;
typedef __attribute__((ext_vector_type(2))) unsigned u32x2;
typedef __attribute__((ext_vector_type(8))) __bf16 bf8v;
typedef __attribute__((ext_vector_type(2))) __bf16 bf2v;
typedef unsigned short u16;
#define LAS __attribute__((address_space(3)))
#define DI __device__ __forceinline__

constexpr int S = 2048, D = 640, DH = 20, F = 1536;
constexpr int REC = 2688, R_K2 = 1024, R_V0 = 1280, R_V1 = 1984, V_H = 336;
constexpr int HC = 8 * REC;
constexpr size_t WS_EPOCH = 0;
constexpr size_t WS_FKV = 4096;
constexpr size_t WS_FFF = WS_FKV + 32768;
constexpr size_t WS_G = WS_FFF + 32768;
constexpr size_t WS_KV = WS_G + S * 4;
constexpr size_t WS_O = WS_KV + 64 * REC + 1024;
constexpr size_t WS_YP = WS_O + (size_t)S * D * 2;
constexpr float QSCALE = 0.22360679774997896f * 1.4426950408889634f;
constexpr int L_W0 = 81920, L_W2 = L_W0 + 2 * 20480, L_RG = L_W2 + 25600, L_WO = 2 * HC, L_MISC = 163840 - 1024, LDS_BYTES = 163840;
constexpr int EXROW = 336, EXH = 64 * EXROW;
constexpr unsigned SPIN_CAP = 1u << 18;

DI float bf2f(u16 b) { return __uint_as_float((unsigned)b << 16); }
DI float bflo(unsigned u) { return __uint_as_float(u << 16); }
DI float bfhi(unsigned u) { return __uint_as_float(u & 0xffff0000u); }
DI unsigned pk2(float lo, float hi) { bf2v v; v[0] = (__bf16)lo; v[1] = (__bf16)hi; return __builtin_bit_cast(unsigned, v); }
DI float rbf(float x) { return (float)(__bf16)x; }
DI f32x4 mfma16(bf16x8 a, bf16x8 b, f32x4 c) { return __builtin_amdgcn_mfma_f32_16x16x32_bf16(__builtin_bit_cast(bf8v, a), __builtin_bit_cast(bf8v, b), c, 0, 0, 0); }
DI f32x16 mfma32(bf16x8 a, bf16x8 b, f32x16 c) { return __builtin_amdgcn_mfma_f32_32x32x16_bf16(__builtin_bit_cast(bf8v, a), __builtin_bit_cast(bf8v, b), c, 0, 0, 0); }
DI f32x16 mfma32k8(bf16x4 a, bf16x4 b, f32x16 c) { return __builtin_amdgcn_mfma_f32_32x32x8bf16_1k(a, b, c, 0, 0, 0); }
constexpr unsigned SPIDX = 0xddddddddu;
DI f32x16 pv32(bf16x8 v, f32x8 p, f32x16 c) {
  return __builtin_amdgcn_smfmac_f32_32x32x32_bf16(__builtin_bit_cast(bf8v, v), __builtin_bit_cast(bf16v, p), c, (int)SPIDX, 0, 0); }
DI bf16x4 trread(unsigned a) { return __builtin_amdgcn_ds_read_tr16_b64_v4i16((LAS bf16x4*)(size_t)a); }
DI bf16x8 trfrag(unsigned a, unsigned b) { const bf16x4 lo = trread(a), hi = trread(b); return __builtin_shufflevector(lo, hi, 0, 1, 2, 3, 4, 5, 6, 7); }
DI bf16x8 lds128(unsigned a) { return *(LAS const bf16x8*)(size_t)a; }
DI float sigmoidf(float x) { return __builtin_amdgcn_rcpf(1.0f + __expf(-x)); }
#define GL(v, o) "global_load_lds_dwordx4 " v ", %1 offset:" #o
#define M0SET "s_mov_b32 m0, %0\n\ts_nop 0\n\t"
DI void dma1(const void* sb, unsigned dst, unsigned v0) { asm volatile(M0SET GL("%2", 0) :: "s"(dst), "s"(sb), "v"(v0) : "memory"); }
DI void dma2(const void* sb, unsigned dst, unsigned v0, unsigned v1) { asm volatile(M0SET GL("%2", 0) "\n\t" GL("%3", 1024) :: "s"(dst), "s"(sb), "v"(v0), "v"(v1) : "memory"); }
DI void dma3(const void* sb, unsigned dst, unsigned v0, unsigned v1, unsigned v2) {
  asm volatile(M0SET GL("%2", 0) "\n\t" GL("%3", 1024) "\n\t" GL("%4", 2048) :: "s"(dst), "s"(sb), "v"(v0), "v"(v1), "v"(v2) : "memory"); }
DI void dma4(const void* sb, unsigned dst, unsigned v0, unsigned v1, unsigned v2, unsigned v3) {
  asm volatile(M0SET GL("%2", 0) "\n\t" GL("%3", 1024) "\n\t" GL("%4", 2048) "\n\t" GL("%5", 3072) :: "s"(dst), "s"(sb), "v"(v0), "v"(v1), "v"(v2), "v"(v3) : "memory"); }
DI void dmaw(const void* sb, unsigned dst, const u32x4& v) {
  asm volatile(M0SET GL("%2", 0) "\n\ts_add_u32 m0, m0, 5120\n\ts_nop 0\n\t" GL("%3", 0) "\n\ts_add_u32 m0, m0, 5120\n\ts_nop 0\n\t" GL("%4", 0)
               "\n\ts_add_u32 m0, m0, 5120\n\ts_nop 0\n\t" GL("%5", 0) :: "s"(dst), "s"(sb), "v"(v[0]), "v"(v[1]), "v"(v[2]), "v"(v[3]) : "scc", "memory"); }
DI void dma1d(const void* sb, unsigned dst, unsigned v) { asm volatile(M0SET GL("%2", 0) " sc1" :: "s"(dst), "s"(sb), "v"(v) : "memory"); }
DI void dma2d(const void* sb, unsigned dst, unsigned v) { asm volatile(M0SET GL("%2", 0) " sc1\n\t" GL("%2", 1024) " sc1" :: "s"(dst), "s"(sb), "v"(v) : "memory"); }
DI void dma4d(const void* sb, unsigned dst, unsigned v) {
  asm volatile(M0SET GL("%2", 0) " sc1\n\t" GL("%2", 1024) " sc1\n\t" GL("%2", 2048) " sc1\n\t" GL("%2", 3072) " sc1" :: "s"(dst), "s"(sb), "v"(v) : "memory"); }
DI void st32_dev(const void* sbase, unsigned voff, unsigned v) { asm volatile("global_store_dword %0, %1, %2 sc1" :: "v"(voff), "v"(v), "s"(sbase) : "memory"); }
DI void st64_dev(const void* sbase, unsigned voff, u32x2 v) { asm volatile("global_store_dwordx2 %0, %1, %2 sc1" :: "v"(voff), "v"(v), "s"(sbase) : "memory"); }
DI void st128_dev(const void* sbase, unsigned voff, u32x4 v) { asm volatile("global_store_dwordx4 %0, %1, %2 sc1\n\ts_nop 1" :: "v"(voff), "v"(v), "s"(sbase) : "memory"); }
DI unsigned ld32_dev(const void* sbase, unsigned voff) { unsigned v; asm volatile("global_load_dword %0, %1, %2 sc1\n\ts_waitcnt vmcnt(0)" : "=&v"(v) : "v"(voff), "s"(sbase) : "memory"); return v; }
DI u32x4 ld128_dev(const void* sbase, unsigned voff) { u32x4 v; asm volatile("global_load_dwordx4 %0, %1, %2 sc1\n\ts_waitcnt vmcnt(0)" : "=&v"(v) : "v"(voff), "s"(sbase) : "memory"); return v; }
DI u32x2 ld64_dev(const void* sbase, unsigned voff) { u32x2 v; asm volatile("global_load_dwordx2 %0, %1, %2 sc1\n\ts_waitcnt vmcnt(0)" : "=&v"(v) : "v"(voff), "s"(sbase) : "memory"); return v; }
#define WAITBAR(n) asm volatile("s_waitcnt vmcnt(" #n ") lgkmcnt(0)\n\ts_barrier" ::: "memory")

DI float tile_max(const f32x16& s) {
  float m = fmaxf(fmaxf(s[0], s[1]), s[2]);
#pragma unroll
  for (int i = 3; i < 15; i += 2) m = fmaxf(fmaxf(m, s[i]), s[i + 1]);
  m = fmaxf(m, s[15]);
  return m;
}
DI float upper_half(float v) {
  return __uint_as_float(__builtin_amdgcn_permlane32_swap(__float_as_uint(v), __float_as_uint(v), false, false)[1]);
}
DI float max_halves(float v) {
  const auto s = __builtin_amdgcn_permlane32_swap(__float_as_uint(v), __float_as_uint(v), false, false);
  return fmaxf(__uint_as_float(s[0]), __uint_as_float(s[1]));
}
DI int wpos(int row) { return (row & ~7) | ((row & 3) << 1) | ((row >> 2) & 1); }
DI unsigned wrow(unsigned pos) { return (pos & ~7u) | ((pos & 1) << 2) | ((pos >> 1) & 3); }
DI unsigned umax(unsigned a, unsigned b) { return a > b ? a : b; }

DI unsigned row_max(unsigned v) {
  v = umax(v, __builtin_amdgcn_update_dpp(0u, v, 0x128, 0xf, 0xf, false)); v = umax(v, __builtin_amdgcn_update_dpp(0u, v, 0x124, 0xf, 0xf, false));
  v = umax(v, __builtin_amdgcn_update_dpp(0u, v, 0x122, 0xf, 0xf, false)); return umax(v, __builtin_amdgcn_update_dpp(0u, v, 0x121, 0xf, 0xf, false));
}
DI u32x4 wphase(unsigned j, unsigned lane) {
  const unsigned u = 64 * j + lane, pos = (u * 205) >> 11, v = wrow(pos) * 1280 + (u - pos * 10) * 16;
  return (u32x4){v, v + 40960, v + 81920, v + 122880};
}
#define SB0() __builtin_amdgcn_sched_barrier(0)
#define LD128D(dst, voff, sbase) asm volatile("global_load_dwordx4 %0, %1, %2 sc1" : "=v"(dst) : "v"(voff), "s"(sbase) : "memory")
#define PIN(x) asm volatile("" : "+v"(x))

constexpr int M_RDY = 16, M_DN = 32, M_HS = 64, M_ZERO = 112, M_WL = 128, M_QD = 160, M_RED = 192, M_H = -64, M_G = 768, L_KP = L_RG + 2560, L_FG = 6 * HC, L_FD = L_FG + 20480, L_YS = L_WO + 102400;
DI void lds_st(unsigned a, unsigned v) { *(volatile LAS unsigned*)(size_t)a = v; }
DI unsigned lds_ld(unsigned a) { return *(volatile LAS unsigned*)(size_t)a; }
template <int SLEEP> DI void lds_wait(unsigned a, unsigned v) {
  unsigned t, n;
  asm volatile("s_mov_b32 %1, 0\n1:\n\tds_read_b32 %0, %2\n\ts_add_u32 %1, %1, 1\n\ts_waitcnt lgkmcnt(0)\n\tv_cmp_gt_u32 vcc, %3, %0\n\ts_cbranch_vccz 2f\n\t"
               "s_cmp_gt_u32 %1, %4\n\ts_cbranch_scc1 2f\n\ts_sleep %5\n\ts_branch 1b\n2:"
               : "=&v"(t), "=&s"(n) : "v"(a), "s"(v), "s"(SPIN_CAP << 2), "i"(SLEEP) : "vcc", "scc", "memory");
}
#define BAR() asm volatile("s_waitcnt lgkmcnt(0)\n\ts_barrier" ::: "memory")
constexpr int TDELAY = 1, TDELAY2 = 8, PDELAY = 10;
#define WAITV(n) asm volatile("s_waitcnt vmcnt(" #n ")" ::: "memory")

__global__ __launch_bounds__(768, 3) void k_fused(const u16* __restrict__ x, const u16* __restrict__ wq, const u16* __restrict__ wk,
                                                  const u16* __restrict__ wv, const u16* __restrict__ wo, const u16* __restrict__ wr,
                                                  const u16* __restrict__ wg, const u16* __restrict__ wu, const u16* __restrict__ wd,
                                                  const u16* __restrict__ gw, char* __restrict__ ws, u16* __restrict__ out) {
  extern __shared__ __attribute__((aligned(16))) char lds[];

  const int b = blockIdx.x, rt = (b & 7) * 4 + ((b >> 3) & 3), cg = b >> 5;
  const int tid = threadIdx.x, lane = tid & 63, wv12 = __builtin_amdgcn_readfirstlane(tid >> 6);
  const int r = lane & 31, h = lane >> 5, c = lane & 15, g = lane >> 4, q4 = c >> 2, p4 = lane & 3;
  const unsigned L = (unsigned)(size_t)(LAS char*)lds, l16 = lane * 16, LM = L + L_MISC;
  const int tkv = cg >> 1, hh = wv12 & 3;
  const bool isv = cg & 1;
  int rot = cg + (rt & 3); rot -= (rot >= 5) ? 5 : 0; rot -= (rot >= 5) ? 5 : 0;
  auto kchunk = [&](int i) { const int k = i + rot; return k >= 5 ? k - 5 : k; };
  const unsigned vX = g * 1280 + ((c ^ g) * 16);
  {
    const bool xw = wv12 < 5;
    const unsigned wo4 = wv12 < 11 ? (wv12 - 5) * 4096 : 21504;
    const unsigned m = (unsigned)((wv12 - 5) >> 31);
    unsigned so = ((rt * 81920 + tkv * 20480 + kchunk(wv12) * 256) & m) | (wo4 & ~m), sd = L + (((wv12 * 16384 + tkv * 4096) & m) | ((L_W2 + wo4) & ~m));
    unsigned v0 = (vX & m) | (l16 & ~m), v1 = v0 ^ (0x1040 & m), v2 = v0 ^ (0x2080 & m), v3 = v0 ^ (0x30c0 & m);
    asm volatile("" : "+s"(so), "+s"(sd), "+v"(v0), "+v"(v1), "+v"(v2), "+v"(v3));
    asm volatile("" :: "s"(x), "s"(wq), "s"(wk), "s"(wv), "s"(wo), "s"(wr), "s"(wg), "s"(wu), "s"(wd), "s"(gw), "s"(ws), "s"(out));
    const unsigned long long px = (unsigned long long)x, pk = (unsigned long long)(isv ? wv : wk);
    const unsigned lo = xw ? (unsigned)px : (unsigned)pk, hi = xw ? (unsigned)(px >> 32) : (unsigned)(pk >> 32);
    dma4((const char*)(((unsigned long long)hi << 32) | lo) + so, sd, v0, v1, v2, v3);
  }
  const char* xt = (const char*)x + rt * 81920;
  auto xdma = [&](int tg, int i) { dma4(xt + tg * 20480 + kchunk(i) * 256, L + i * 16384 + tg * 4096, vX, (vX ^ 64) + 4096, (vX ^ 128) + 8192, (vX ^ 192) + 12288); };
  const char* kvg = ws + WS_KV;
  const char* og = ws + WS_O + rt * 81920;
  const int hc0 = (b >> 2) & 1;

  if (wv12 == 11) {
    dma1(wr, L + L_RG, l16); dma1(gw, L + L_RG + 1280, l16);
    if (lane < 16) { dma1((const char*)wr + 1024, L + L_RG + 1024, l16); dma1((const char*)gw + 1024, L + L_RG + 2304, l16); }
  }
  if (wv12 == 0) lds_st(LM + lane * 4, 0);
  if (__builtin_expect(wv12 < 4, 1)) {
  const int w = wv12;
  __builtin_amdgcn_s_setprio(3);
  const unsigned ardy = LM + M_RDY + w * 4, adn = LM + M_DN + (lane & 7) * 4, ahs = LM + M_HS + p4 * 4, aqd = LM + M_QD + (lane & 7) * 4;
  const unsigned aks = LM + M_WL + 12 + (lane < 5 ? lane : 0) * 4;
  auto kvdma = [&](int j, int slot) {
    const char* src = kvg + ((hc0 + j) & 7) * HC + w * 5120;
    const unsigned dst = L + slot * HC + w * 5120;
    if (w == 3) dma2d(src + 4096, dst + 4096, l16); else dma1d(src + 4096, dst + 4096, l16);
    dma4d(src, dst, l16);
  };
  u32x4 vA = wphase(w, lane), vB;
  unsigned tag, gv = 0;
  if (__builtin_expect_with_probability(w < 3, 1, 0.6)) {
    vB = wphase(3 + (w & 1), lane);
    const int tg = w + (w >= tkv ? 1 : 0);
    const unsigned awl = LM + M_WL + w * 4;

#pragma unroll 1
    for (int i = 0; i < 5; ++i) {
      xdma(tg, i);
      if (i >= 2) lds_wait<1>(i == 2 ? aks : aqd, i == 2 ? 1 : i - 2);
      const char* src = (const char*)wq + cg * 160 + kchunk(i) * (128 * 1280);
      const unsigned dst = L + L_W0 + (i >= 3 ? i - 3 : i) * 20480 + w * 1024;
      dmaw(src, dst, vA);
      if (w < 2) { dmaw(src, dst + 3072, vB); WAITV(12); } else WAITV(8);
      if (i == 0) BAR(); else lds_st(awl, i);

    }

    if (__builtin_amdgcn_readfirstlane(lds_ld(LM + 4))) { kvdma(0, 0); WAITV(5); lds_st(awl, 5); }
    else { WAITV(0); lds_st(awl, 5); lds_wait<1>(LM + 4, 1); kvdma(0, 0); }

    tag = __builtin_amdgcn_readfirstlane(lds_ld(LM));
  } else {
    const int T = tkv * 16 + (rt >> 1), sub = rt & 1, tok = rt * 64 + tkv * 16 + c;
    unsigned sA, sB, sD;
    {
      const unsigned hv = lane >= 21 ? 1 : 0, d = lane - 21 * hv;
      const bool t0 = isv ? d < 16 : lane < 32;
      const unsigned i0 = isv ? hv * 32 + d : lane, i1 = isv ? hv * 8 + (d & 3) : (lane & 7) * 2;
      sA = t0 ? i0 * 16 : 1024 + i1 * 16; sB = sA + (t0 ? (isv ? 256 : 512) : isv ? 64 : 16);
      const unsigned dk = t0 ? (g * 32 + sub * 16 + c) * 16 : R_K2 + sub * 128 + (lane & 7) * 16;
      sD = (isv ? l16 : dk) + ((T - (isv ? 1 - sub : 2)) & 63) * REC + (isv ? R_V0 + sub * (R_V1 - R_V0) : 0);
    }
    unsigned fo;
    { const int n = hc0 * 16 + (lane < 40 ? lane >> 1 : 0), prt = n & 31; fo = ((prt >> 2) + (prt & 3) * 8 + ((n >> 5) * 2 + (lane & 1)) * 32) * 128; }
    sA += L + L_KP; sB += L + L_KP;
    unsigned sR = L + L_KP + 1280 + c * 16;
    float ks = isv ? 1.0f : QSCALE;
    PIN(sA); PIN(sB); PIN(sD); PIN(fo); PIN(sR); PIN(ks);
    { unsigned e; asm volatile("global_load_dword %0, %1, %2 sc1\n\ts_waitcnt vmcnt(1)\n\ts_barrier\n\ts_waitcnt vmcnt(0)" : "=&v"(e) : "v"(0), "s"(ws + WS_EPOCH) : "memory");
      tag = __builtin_amdgcn_readfirstlane(e); }
    tag = tag * 0x9E3779B1u + 0x7F4A7C15u;

    lds_wait<0>(aks, 1);

    f32x4 pa[5], pb[5]; f32x2 pr[5];
#pragma unroll
    for (int i = 0; i < 5; ++i) { pa[i] = *(volatile LAS const f32x4*)(size_t)(sA + i * 1536); pb[i] = *(volatile LAS const f32x4*)(size_t)(sB + i * 1536); }
#pragma unroll
    for (int i = 0; i < 5; ++i) pr[i] = *(volatile LAS const f32x2*)(size_t)(sR + i * 1536);
    const f32x4 ea = ((pa[0] + pa[1]) + (pa[2] + pa[3])) + pa[4], eb = ((pb[0] + pb[1]) + (pb[2] + pb[3])) + pb[4];
    u32x4 o = {pk2(ea[0] * ks, ea[1] * ks), pk2(ea[2] * ks, ea[3] * ks), pk2(eb[0] * ks, eb[1] * ks), pk2(eb[2] * ks, eb[3] * ks)};
    const bool ones = isv & ((lane == 20) | (lane == 41));
#pragma unroll
    for (int i = 0; i < 4; ++i) o[i] = ones ? 0x3f803f80u : o[i];
    if (lane < (isv ? 44 : 40)) st128_dev(kvg, sD, o);

    const f32x2 e = ((pr[0] + pr[1]) + (pr[2] + pr[3])) + pr[4];
    const unsigned rb = pk2(e[0], 0.f) & 0xffffu;
    unsigned k = (rb & 0x8000u) ? (~rb & 0xffffu) : (rb | 0x8000u);
    if ((rb & 0x7fffu) > 0x7f80u) k = 0;
    k = row_max((k << 16) | (unsigned)(2047 - tok));
    if (isv) k = 0;
    gv = (pk2(sigmoidf(rbf(e[1])), 0.f) << 16) | (tag & 0xffffu);
    lds_st(LM, tag);
    PIN(k);
    WAITV(0);
    if (lane == 0) st64_dev(ws + WS_FKV, b * 128, (u32x2){tag, k});

    WAITV(0);
    __builtin_amdgcn_s_sleep(PDELAY);
    for (unsigned spins = 0; !__all(ld32_dev(ws + WS_FKV, fo) == tag) && spins < SPIN_CAP; ++spins) __builtin_amdgcn_s_sleep(2);
    { const char* pre = kvg + ((hc0 * 8 - 2) & 63) * REC;
      dma1d(pre, L + L_KP, l16); if (lane < 16) dma1d(pre + 1024, L + L_KP + 1024, l16);
      dma2d(pre + REC, L + L_KP + REC, l16); }
    lds_st(LM + 4, 1);

    lds_wait<1>(aqd, 2);
    kvdma(0, 0);
    WAITV(6); lds_st(LM + 8, 1);
  }
  unsigned v4 = wphase(4, lane)[0] + w * 40960;
  unsigned hF = lane * (F * 2), hF2 = (lane + 64) * (F * 2) - 1024, hX = g * 16, hK = (w * 64 + lane) * 128;
  unsigned fa = L + L_FG + (p4 >> 1) * 10240 + (w * 160 + 8 * g + q4) * 16 + (p4 & 1) * 8, da = L + L_FD + q4 * 1280 + (w - 1) * 128 + g * 32 + p4 * 8;
  unsigned hR = LM + M_RED + 64 + w * 64 + g * 16, hS = LM + M_RED + 64 + (lane & 7) * 4, hH = LM + M_H + (lane & 7) * 2, hH2 = c == 4 * g ? LM + M_H : LM + M_ZERO;
  unsigned hY = (b * D + (w - 1) * 64 + lane) * 4, hN = ((b & 31) + 32 * (r >> 2)) * 128 + p4 * 4, hHS = LM + M_HS + w * 4, hRD = ardy, hDN = adn, hAS = ahs, hA1 = LM + M_HS + (p4 ? p4 : 1) * 4;
  u32x4 mark = {~0u, ~0u, ~0u, ~0u};
  asm volatile("" : "+v"(v4), "+v"(hF), "+v"(hF2), "+v"(hX), "+v"(hK), "+v"(fa), "+v"(da), "+v"(hR), "+v"(hS), "+v"(hH), "+v"(hH2), "+v"(hY), "+v"(hN),
               "+v"(hHS), "+v"(hRD), "+v"(hDN), "+v"(hAS), "+v"(hA1), "+v"(mark));
  auto wdma = [&](const char* src, unsigned dst) { dmaw(src, dst + w * 1024, vA); dma1(src, dst + 4096 + w * 5120, v4); };
  WAITV(0); lds_st(hRD, 2);
  unsigned key;
  for (unsigned spins = 0;; ++spins) {
    const u32x2 f = ld64_dev(ws + WS_FKV, hK);
    key = f[1];
    if (__all(f[0] == tag) || spins > SPIN_CAP) break;
  }
  key = row_max(key);
  if (c == 0) lds_st(LM + M_RED + (w * 4 + g) * 4, key);
  lds_st(hHS, 1);
  lds_wait<1>(hAS, 1);
  lds_wait<1>(aqd, 5);

  kvdma(1, 1); kvdma(2, 2); WAITV(5); lds_st(hRD, 3);
  kvdma(3, 3); WAITV(5); lds_st(hRD, 4);
  key = __builtin_amdgcn_readfirstlane(row_max(lds_ld(LM + M_RED + c * 4)));
  const int sel = 2047 - (int)(key & 2047u);
  kvdma(4, 4); WAITV(5); lds_st(hRD, 5);
  kvdma(5, 5); WAITV(5); lds_st(hRD, 6);
  WAITV(0); lds_st(hRD, 7);
  if (w == 3 && !isv && g == 0) st32_dev(ws + WS_G, (rt * 64 + tkv * 16 + c) * 4, gv);
  if (b == 0 && tid == 0) st32_dev(ws + WS_EPOCH, 0, tag);
  { const char* od = og + cg * 10240 + w * 1024;
    st128_dev(od, l16, mark); st128_dev(od + 4096, l16, mark); if (w < 2) st128_dev(od + 8192, l16, mark); }

  if (b < 192) {
    lds_wait<8>(hDN, 1);
    const int i3 = b >> 3, f0 = ((b & 7) * 3 + (i3 >> 3)) * 64 + (i3 & 7) * 8;
#pragma unroll
    for (int m = 0; m < 2; ++m) {
      const char* src = (const char*)(m ? wu : wg) + (w * 160 * F + f0) * 2;
      const unsigned dst = L + L_FG + m * 10240 + w * 2560;
      dma2(src, dst, hF, hF2);
      if (lane < 32) dma1(src + 128 * F * 2, dst + 2048, hF);
    }
    { const char* src = (const char*)(wd + f0 * D) + w * 3072;
      if (w < 3) dma3(src, L + L_FD + w * 3072, l16, l16, l16); else dma1(src, L + L_FD + w * 3072, l16); }
    bf16x8 xs[5];
#pragma unroll
    for (int i = 0; i < 5; ++i) asm volatile("global_load_dwordx4 %0, %1, %2 offset:%3" : "=v"(xs[i]) : "v"(hX), "s"(x + sel * D + w * 160), "i"(i * 64) : "memory");
    asm volatile("s_waitcnt vmcnt(0)" : "+v"(xs[0]), "+v"(xs[1]), "+v"(xs[2]), "+v"(xs[3]), "+v"(xs[4]) :: "memory");
    f32x4 a = {0.f, 0.f, 0.f, 0.f};
#pragma unroll
    for (int i = 0; i < 5; ++i) a = mfma16(trfrag(fa + i * 512, fa + i * 512 + 64), xs[i], a);
    *(volatile LAS f32x4*)(size_t)hR = a;
    if (w == 0) {
      lds_wait<4>(hA1, 3);
      float sv[8];
#pragma unroll
      for (int i = 0; i < 8; ++i) sv[i] = __uint_as_float(lds_ld(hS + i * 32));
      const float gs = rbf((sv[0] + sv[2]) + (sv[4] + sv[6])), us = rbf((sv[1] + sv[3]) + (sv[5] + sv[7]));
      *(volatile LAS u16*)(size_t)hH = (u16)pk2(rbf(gs * sigmoidf(gs)) * us, 0.f);
      lds_st(hHS, 3);
    } else {
      lds_st(hHS, 3);
      lds_wait<4>(hAS, 3);
      const bf16x8 hf = *(volatile LAS const bf16x8*)(size_t)hH2;
#pragma unroll
      for (int t = 0; t < 4; ++t) {
        if (t == 3 && w != 1) break;
        const f32x4 y = mfma16(hf, trfrag(da + t * 384, da + t * 384 + 5120), (f32x4){0.f, 0.f, 0.f, 0.f});
        __hip_atomic_store((float*)(ws + WS_YP + t * 768 + hY), y[0], __ATOMIC_RELAXED, __HIP_MEMORY_SCOPE_AGENT);
      }
    }
  }
  WAITV(0);
  if (lane == 0) st32_dev(ws + WS_FFF, b * 128 + w * 4, tag);

  lds_wait<16>(hDN, 2);
  kvdma(6, 0); kvdma(7, 1); WAITV(0); lds_st(hRD, 9);

  lds_wait<16>(hDN, 4);
  wdma((const char*)wo + cg * 160, L + L_WO); wdma((const char*)wo + cg * 160 + 128 * 1280, L + L_WO + 20480);
  const bool owner = (sel >> 6) == rt;
  for (unsigned spins = 0; spins < SPIN_CAP; ++spins) {
    unsigned bad;
    if (owner) { const u32x4 f0 = ld128_dev(ws + WS_FFF, hK); bad = (f0[0] ^ tag) | (f0[1] ^ tag) | (f0[2] ^ tag) | (f0[3] ^ tag); }
    else bad = ld32_dev(ws + WS_FFF, hN) ^ tag;
    if (__all(bad == 0)) break;
    __builtin_amdgcn_s_sleep(8);
  }
  if (owner) { lds_st(hHS, 4); lds_wait<2>(hAS, 4); }
  dma1(xt + cg * 160, L + L_FD + w * 1024, vA[0]); dma1(xt + cg * 160, L + L_FD + w * 1024 + 5120, vA[1]);
  if (w < 2) dma1(xt + cg * 160, L + L_FD + 4096 + w * 5120, v4);
  if (w == 3) {
    for (unsigned spins = 0; gv = ld32_dev(ws + WS_G, (rt * 64 + lane) * 4), !__all(((gv ^ tag) & 0xffffu) == 0) && spins < SPIN_CAP; ++spins) {}
    lds_st(LM + M_G + lane * 4, gv & 0xffff0000u);
  }
  if (owner) {
    const int cu = tid % 20, pr = tid / 20;
    if (tid < 240) {
      f32x4 yv[16];
#pragma unroll
      for (int i = 0; i < 16; ++i) LD128D(yv[i], (pr * 16 * D + cg * 80 + cu * 4) * 4, ws + WS_YP + i * D * 4);
      asm volatile("s_waitcnt vmcnt(0)" : "+v"(yv[0]), "+v"(yv[1]), "+v"(yv[2]), "+v"(yv[3]), "+v"(yv[4]), "+v"(yv[5]), "+v"(yv[6]), "+v"(yv[7]),
                   "+v"(yv[8]), "+v"(yv[9]), "+v"(yv[10]), "+v"(yv[11]), "+v"(yv[12]), "+v"(yv[13]), "+v"(yv[14]), "+v"(yv[15]) :: "memory");
#pragma unroll
      for (int st = 8; st >= 1; st >>= 1)
#pragma unroll
        for (int i = 0; i < st; ++i) yv[i] += yv[i + st];
      *(volatile LAS f32x4*)(size_t)(L + L_YS + (pr * 80 + cu * 4) * 4) = yv[0];
    }
    lds_st(hHS, 5);
    lds_wait<2>(hAS, 5);
    if (tid < 80) {
      float y = 0.f;
#pragma unroll
      for (int p = 0; p < 12; ++p) y += __uint_as_float(lds_ld(L + L_YS + (p * 80 + tid) * 4));
      const unsigned kb = key >> 16;
      const float rsel = rbf(sigmoidf(bf2f((u16)((kb & 0x8000u) ? (kb & 0x7fffu) : (~kb & 0xffffu)))));
      volatile LAS u16* xp = (volatile LAS u16*)(size_t)(L + L_FD + wpos(sel & 63) * 160 + tid * 2);
      *xp = (u16)pk2(bf2f(*xp) + rbf(rbf(y) * rsel), 0.f);
    }
  }

  lds_wait<10>(hDN, 6);
#pragma unroll
  for (int i = 2; i < 5; ++i) wdma((const char*)wo + cg * 160 + i * (128 * 1280), L + L_WO + i * 20480);
  WAITV(0); lds_st(hRD, 10);

  return;
  }
  {
    const int w = wv12 - 4, th = w >> 2;
    __builtin_amdgcn_s_setprio(3);
    const unsigned qX = L + (32 * th + r) * 256;
    const unsigned qW = ((g & 1) && p4 ? 0x40000u : L + L_W0) + (8 * h + 2 * q4) * 160 + 40 * hh + (g & 1) * 32 + 8 * p4;
    f32x16 zero;
#pragma unroll
    for (int i = 0; i < 16; ++i) zero[i] = 0.f;
    f32x16 qa = zero;
    const unsigned ok2 = R_K2 + r * 8, ov = R_V0 + h * V_H + r * 16;
    const int tt = w & 3, kh = w >> 2;
    const unsigned ardy = LM + M_RDY + p4 * 4;
    unsigned kvseen;
    unsigned ok2l = h ? R_V0 + 20 * 16 : ok2;
    unsigned ovl = r > 20 ? 0x40000u : ov;
    PIN(ok2l); PIN(ovl);
    unsigned oa1, oa2;
    { const int ks = 2 * cg + (hh >> 1), t2 = 2 * th + (r >> 4);
      oa1 = (t2 * 20 + (ks >> 3) * 10 + (ks & 7)) * 1024 + ((2 * (hh & 1) + h) * 16 + c) * 16;
      oa2 = (t2 * 20 + (cg >> 2) * 10 + 8 + ((cg >> 1) & 1)) * 1024 + ((cg & 1) * 2 + (hh & 1)) * 256 + (hh >> 1) * 128 + c * 8; }
    PIN(oa1); PIN(oa2);
    f32x16 o = zero;
    f32x8 p1 = {0.f, 0.f, 0.f, 0.f, 0.f, 0.f, 0.f, 0.f};
    bf16x8 vf1 = {0, 0, 0, 0, 0, 0, 0, 0};
    asm volatile("" : "+{v[0:15]}"(o), "+{v[64:71]}"(p1), "+{v[84:87]}"(vf1));
    typedef __attribute__((ext_vector_type(8))) unsigned u32x8;
    u32x8 ax; unsigned aw = qW, awl = LM + M_WL + (p4 < 3 ? p4 : 0) * 4, aqd = LM + M_QD + w * 4, akv = LM + 8;
#pragma unroll
    for (int s = 0; s < 8; ++s) ax[s] = qX + (((2 * s + h) ^ c) * 16);
    asm volatile("" : "+{v[16:31]}"(qa), "+{v[32:39]}"(ax), "+{v40}"(aw), "+v"(awl), "+v"(aqd), "+v"(akv));
    if (w < 5) {
      const int kc = kchunk(w);
      unsigned aX = L + w * 16384 + (16 * tkv + c) * 256, aW = L + L_W2 + kc * 5120 + (8 * g + q4) * 40, aR = L + L_RG + kc * 256 + (c & 1) * 1280 + g * 16;
      unsigned aP = L + L_KP + w * 1536 + l16, aP1 = L + L_KP + w * 1536 + 1024 + (isv ? g * 4 + p4 : c) * 16, aP2 = L + L_KP + w * 1536 + 1280 + c * 16;
      asm volatile(
      "s_waitcnt vmcnt(0)\n"
      "s_barrier\n"
      "ds_read_b128 v[44:47], %[x0]\n"
      "ds_read_b64_tr_b16 v[96:97], %[w0] offset:0\n"
      "ds_read_b64_tr_b16 v[98:99], %[w0] offset:160\n"
      "ds_read_b64_tr_b16 v[112:113], %[w1] offset:32\n"
      "ds_read_b64_tr_b16 v[114:115], %[w1] offset:192\n"
      "ds_read_b128 v[48:51], %[x1]\n"
      "ds_read_b64_tr_b16 v[100:101], %[w0] offset:1280\n"
      "ds_read_b64_tr_b16 v[102:103], %[w0] offset:1440\n"
      "ds_read_b64_tr_b16 v[116:117], %[w1] offset:1312\n"
      "ds_read_b64_tr_b16 v[118:119], %[w1] offset:1472\n"
      "ds_read_b128 v[52:55], %[x2]\n"
      "ds_read_b64_tr_b16 v[104:105], %[w0] offset:2560\n"
      "ds_read_b64_tr_b16 v[106:107], %[w0] offset:2720\n"
      "ds_read_b64_tr_b16 v[120:121], %[w1] offset:2592\n"
      "ds_read_b64_tr_b16 v[122:123], %[w1] offset:2752\n"
      "ds_read_b128 v[56:59], %[x3]\n"
      "ds_read_b64_tr_b16 v[108:109], %[w0] offset:3840\n"
      "ds_read_b64_tr_b16 v[110:111], %[w0] offset:4000\n"
      "ds_read_b64_tr_b16 v[124:125], %[w1] offset:3872\n"
      "ds_read_b64_tr_b16 v[126:127], %[w1] offset:4032\n"
      "s_cmp_lg_u32 %[isv], 0\n"
      "s_cbranch_scc1 1f\n"
      "ds_read_b128 v[60:63], %[rg] offset:0\n"
      "ds_read_b128 v[72:75], %[rg] offset:64\n"
      "ds_read_b128 v[76:79], %[rg] offset:128\n"
      "ds_read_b128 v[80:83], %[rg] offset:192\n"
      "s_waitcnt lgkmcnt(15)\n"
      "v_mfma_f32_16x16x32_bf16 v[128:131], v[96:99], v[44:47], 0\n"
      "v_mfma_f32_16x16x32_bf16 v[132:135], v[112:115], v[44:47], 0\n"
      "s_waitcnt lgkmcnt(14)\n"
      "v_mfma_f32_16x16x32_bf16 v[128:131], v[100:103], v[48:51], v[128:131]\n"
      "v_mfma_f32_16x16x32_bf16 v[132:135], v[116:119], v[48:51], v[132:135]\n"
      "s_waitcnt lgkmcnt(9)\n"
      "v_mfma_f32_16x16x32_bf16 v[128:131], v[104:107], v[52:55], v[128:131]\n"
      "v_mfma_f32_16x16x32_bf16 v[132:135], v[120:123], v[52:55], v[132:135]\n"
      "s_waitcnt lgkmcnt(4)\n"
      "v_mfma_f32_16x16x32_bf16 v[128:131], v[108:111], v[56:59], v[128:131]\n"
      "v_mfma_f32_16x16x32_bf16 v[132:135], v[124:127], v[56:59], v[132:135]\n"
      "s_waitcnt lgkmcnt(0)\n"
      "v_mfma_f32_16x16x32_bf16 v[136:139], v[60:63], v[44:47], 0\n"
      "v_mfma_f32_16x16x32_bf16 v[136:139], v[72:75], v[48:51], v[136:139]\n"
      "v_mfma_f32_16x16x32_bf16 v[136:139], v[76:79], v[52:55], v[136:139]\n"
      "v_mfma_f32_16x16x32_bf16 v[136:139], v[80:83], v[56:59], v[136:139]\n"
      "s_nop 2\n"
      "ds_write_b128 %[p0], v[128:131]\n"
      "ds_write_b128 %[p1], v[132:135]\n"
      "s_nop 2\n"
      "ds_write_b128 %[p2], v[136:139]\n"
      "s_branch 2f\n"
      "1:\n"
      "s_waitcnt lgkmcnt(15)\n"
      "v_mfma_f32_16x16x32_bf16 v[128:131], v[44:47], v[96:99], 0\n"
      "v_mfma_f32_16x16x32_bf16 v[132:135], v[44:47], v[112:115], 0\n"
      "s_waitcnt lgkmcnt(10)\n"
      "v_mfma_f32_16x16x32_bf16 v[128:131], v[48:51], v[100:103], v[128:131]\n"
      "v_mfma_f32_16x16x32_bf16 v[132:135], v[48:51], v[116:119], v[132:135]\n"
      "s_waitcnt lgkmcnt(5)\n"
      "v_mfma_f32_16x16x32_bf16 v[128:131], v[52:55], v[104:107], v[128:131]\n"
      "v_mfma_f32_16x16x32_bf16 v[132:135], v[52:55], v[120:123], v[132:135]\n"
      "s_waitcnt lgkmcnt(0)\n"
      "v_mfma_f32_16x16x32_bf16 v[128:131], v[56:59], v[108:111], v[128:131]\n"
      "v_mfma_f32_16x16x32_bf16 v[132:135], v[56:59], v[124:127], v[132:135]\n"
      "s_nop 6\n"
      "ds_write_b128 %[p0], v[128:131]\n"
      "ds_write_b128 %[p1], v[132:135]\n"
      "2:\n"
      "ds_write_b32 %[ks], %[c100]\n"
        :: [x0] "v"(aX + ((g ^ c) * 16)), [x1] "v"(aX + (((4 + g) ^ c) * 16)), [x2] "v"(aX + (((8 + g) ^ c) * 16)), [x3] "v"(aX + (((12 + g) ^ c) * 16)),
           [w0] "v"(aW + p4 * 8), [w1] "v"(aW), [rg] "v"(aR), [p0] "v"(aP), [p1] "v"(aP1), [p2] "v"(aP2), [ks] "v"(LM + M_WL + 12 + w * 4), [c100] "v"(100),
           [isv] "s"(cg & 1)
        : "v44", "v45", "v46", "v47", "v48", "v49", "v50", "v51", "v52", "v53", "v54", "v55", "v56", "v57", "v58", "v59", "v60", "v61", "v62", "v63", "v72", "v73", "v74", "v75", "v76", "v77", "v78", "v79", "v80", "v81", "v82", "v83", "v96", "v97", "v98", "v99", "v100", "v101", "v102", "v103", "v104", "v105", "v106", "v107", "v108", "v109", "v110", "v111", "v112", "v113", "v114", "v115", "v116", "v117", "v118", "v119", "v120", "v121", "v122", "v123", "v124", "v125", "v126", "v127", "v128", "v129", "v130", "v131", "v132", "v133", "v134", "v135", "v136", "v137", "v138", "v139", "scc", "memory");
    } else {
      WAITBAR(0);
    }

    {
      unsigned si, sn;
      asm volatile(
      "s_mov_b32 %[i], 1\n"
      "v_mov_b32 %[seen], 0\n"
      "0:\n"
      "v_cmp_gt_u32 vcc, %[i], %[seen]\n"
      "s_cbranch_vccz 2f\n"
      "s_mov_b32 %[n], 0\n"
      "1:\n"
      "ds_read_b32 %[seen], %[awl]\n"
      "s_add_u32 %[n], %[n], 1\n"
      "s_waitcnt lgkmcnt(0)\n"
      "v_cmp_gt_u32 vcc, %[i], %[seen]\n"
      "s_cbranch_vccz 2f\n"
      "s_cmp_gt_u32 %[n], %[cap]\n"
      "s_cbranch_scc1 2f\n"
      "s_sleep 1\n"
      "s_branch 1b\n"
      "2:\n"
      "ds_read_b64_tr_b16 v[96:97], v40 offset:0\n"
      "ds_read_b64_tr_b16 v[98:99], v40 offset:160\n"
      "ds_read_b128 v[44:47], v32\n"
      "ds_read_b64_tr_b16 v[100:101], v40 offset:2560\n"
      "ds_read_b64_tr_b16 v[102:103], v40 offset:2720\n"
      "ds_read_b128 v[48:51], v33\n"
      "ds_read_b64_tr_b16 v[104:105], v40 offset:5120\n"
      "ds_read_b64_tr_b16 v[106:107], v40 offset:5280\n"
      "ds_read_b128 v[52:55], v34\n"
      "ds_read_b64_tr_b16 v[108:109], v40 offset:7680\n"
      "ds_read_b64_tr_b16 v[110:111], v40 offset:7840\n"
      "ds_read_b128 v[56:59], v35\n"
      "ds_read_b64_tr_b16 v[112:113], v40 offset:10240\n"
      "ds_read_b64_tr_b16 v[114:115], v40 offset:10400\n"
      "ds_read_b128 v[60:63], v36\n"
      "s_waitcnt lgkmcnt(12)\n"
      "v_mfma_f32_32x32x16_bf16 v[16:31], v[96:99], v[44:47], v[16:31]\n"
      "ds_read_b64_tr_b16 v[116:117], v40 offset:12800\n"
      "ds_read_b64_tr_b16 v[118:119], v40 offset:12960\n"
      "ds_read_b128 v[72:75], v37\n"
      "s_waitcnt lgkmcnt(12)\n"
      "v_mfma_f32_32x32x16_bf16 v[16:31], v[100:103], v[48:51], v[16:31]\n"
      "ds_read_b64_tr_b16 v[120:121], v40 offset:15360\n"
      "ds_read_b64_tr_b16 v[122:123], v40 offset:15520\n"
      "ds_read_b128 v[76:79], v38\n"
      "s_waitcnt lgkmcnt(12)\n"
      "v_mfma_f32_32x32x16_bf16 v[16:31], v[104:107], v[52:55], v[16:31]\n"
      "ds_read_b64_tr_b16 v[124:125], v40 offset:17920\n"
      "ds_read_b64_tr_b16 v[126:127], v40 offset:18080\n"
      "ds_read_b128 v[80:83], v39\n"
      "s_waitcnt lgkmcnt(12)\n"
      "v_mfma_f32_32x32x16_bf16 v[16:31], v[108:111], v[56:59], v[16:31]\n"
      "ds_read_b32 %[seen], %[awl]\n"
      "v_mov_b32 v41, %[i]\n"
      "s_cmp_eq_u32 %[i], 3\n"
      "s_cselect_b32 %[n], 0xffff6000, %[ws]\n"
      "s_waitcnt lgkmcnt(10)\n"
      "v_mfma_f32_32x32x16_bf16 v[16:31], v[112:115], v[60:63], v[16:31]\n"
      "v_add_u32 v32, 0x4000, v32\n"
      "v_add_u32 v33, 0x4000, v33\n"
      "s_waitcnt lgkmcnt(7)\n"
      "v_mfma_f32_32x32x16_bf16 v[16:31], v[116:119], v[72:75], v[16:31]\n"
      "v_add_u32 v34, 0x4000, v34\n"
      "v_add_u32 v35, 0x4000, v35\n"
      "s_waitcnt lgkmcnt(4)\n"
      "v_mfma_f32_32x32x16_bf16 v[16:31], v[120:123], v[76:79], v[16:31]\n"
      "v_add_u32 v36, 0x4000, v36\n"
      "v_add_u32 v37, 0x4000, v37\n"
      "s_waitcnt lgkmcnt(1)\n"
      "ds_write_b32 %[aqd], v41\n"
      "v_mfma_f32_32x32x16_bf16 v[16:31], v[124:127], v[80:83], v[16:31]\n"
      "v_add_u32 v38, 0x4000, v38\n"
      "v_add_u32 v39, 0x4000, v39\n"
      "v_add_u32 v40, %[n], v40\n"
      "s_add_u32 %[i], %[i], 1\n"
      "s_cmp_lt_u32 %[i], 6\n"
      "s_waitcnt lgkmcnt(1)\n"
      "s_cbranch_scc1 0b\n"
      "ds_read_b32 %[seen], %[ardy]\n"
      "s_waitcnt lgkmcnt(0)\n"
      "s_nop 7\n"
        : "+{v[16:31]}"(qa), "+{v[32:39]}"(ax), "+{v40}"(aw), [seen] "=&v"(kvseen), [i] "=&s"(si), [n] "=&s"(sn)
        : [awl] "v"(awl), [aqd] "v"(aqd), [ardy] "v"(akv), [cap] "s"(SPIN_CAP << 2), [ws] "s"(20480)
        : "v41", "v44", "v45", "v46", "v47", "v48", "v49", "v50", "v51", "v52", "v53", "v54", "v55", "v56", "v57", "v58", "v59", "v60", "v61", "v62", "v63", "v72", "v73", "v74", "v75", "v76", "v77", "v78", "v79", "v80", "v81", "v82", "v83", "v96", "v97", "v98", "v99", "v100", "v101", "v102", "v103", "v104", "v105", "v106", "v107", "v108", "v109", "v110", "v111", "v112", "v113", "v114", "v115", "v116", "v117", "v118", "v119", "v120", "v121", "v122", "v123", "v124", "v125", "v126", "v127", "vcc", "scc", "memory");

    }
    bf16x8 q1; bf16x4 q2;
    { const u32x4 a = {pk2(qa[0], qa[1]), pk2(qa[2], qa[3]), pk2(qa[4], qa[5]), pk2(qa[6], qa[7])};
      u32x2 a2 = {pk2(qa[8], qa[9]), pk2(qa[10], qa[11])};
      if (h) a2 = (u32x2){0u, 0u};
      q1 = __builtin_bit_cast(bf16x8, a); q2 = __builtin_bit_cast(bf16x4, a2); }

    if (!__all(kvseen >= 1)) lds_wait<0>(LM + 8, 1);
    kvseen = lds_ld(ardy);

    f32x16 s0 = mfma32(lds128(L + L_KP + l16), q1, zero);
    s0 = mfma32k8(*(LAS const bf16x4*)(size_t)(L + L_KP + ok2), q2, s0);
    bf16x8 kf1 = lds128(L + L_KP + REC + l16), vf0 = lds128(L + L_KP + REC + ovl);
    bf16x4 kf2 = *(LAS const bf16x4*)(size_t)(L + L_KP + REC + ok2l);
    float m0 = tile_max(s0); m0 = rbf(max_halves(m0));
#pragma unroll
    for (int i = 0; i < 16; ++i) s0[i] -= m0;
    bf16x4 q2m = q2;
    if (h) q2m = __builtin_bit_cast(bf16x4, (u32x2){pk2(-m0, 0.f), 0u});
    PIN(s0); PIN(q2m);
    if (!__all(kvseen >= 2)) lds_wait<0>(ardy, 2);

    {
      unsigned rdy, dnv, sj, sn, ak = L + l16, ak2 = L + ok2l, av = L + ovl;
      asm volatile(
      "s_mov_b32 %[j], 2\n"
      "v_mov_b32 %[dnv], 1\n"
      "0:\n"
      "s_setprio 3\n"
      "s_waitcnt lgkmcnt(1)\n"
      "v_mfma_f32_32x32x16_bf16 v[48:63], v[72:75], v[88:91], 0\n"
      "ds_read_b128 v[72:75], v92 offset:0\n"
      "v_exp_f32 v32, v32\n"
      "v_exp_f32 v33, v33\n"
      "v_exp_f32 v34, v34\n"
      "v_exp_f32 v35, v35\n"
      "v_smfmac_f32_32x32x32_bf16 v[0:15], v[84:87], v[64:71], v95\n"
      "ds_read_b128 v[84:87], v94 offset:704\n"
      "v_exp_f32 v36, v36\n"
      "v_exp_f32 v37, v37\n"
      "v_exp_f32 v38, v38\n"
      "v_exp_f32 v39, v39\n"
      "v_mfma_f32_32x32x8_bf16 v[48:63], v[76:77], v[78:79], v[48:63]\n"
      "ds_read_b64 v[76:77], v93 offset:0\n"
      "v_exp_f32 v64, v40\n"
      "v_exp_f32 v65, v41\n"
      "v_exp_f32 v66, v42\n"
      "v_exp_f32 v67, v43\n"
      "s_waitcnt lgkmcnt(3)\n"
      "v_smfmac_f32_32x32x32_bf16 v[0:15], v[80:83], v[32:39], v95\n"
      "ds_write_b32 %[adn], %[dnv]\n"
      "ds_read_b128 v[80:83], v94 offset:0\n"
      "v_exp_f32 v68, v44\n"
      "v_exp_f32 v69, v45\n"
      "v_exp_f32 v70, v46\n"
      "v_exp_f32 v71, v47\n"
      "s_waitcnt lgkmcnt(1)\n"
      "v_mfma_f32_32x32x16_bf16 v[32:47], v[72:75], v[88:91], 0\n"
      "ds_read_b128 v[72:75], v92 offset:2688\n"
      "v_exp_f32 v48, v48\n"
      "v_exp_f32 v49, v49\n"
      "v_exp_f32 v50, v50\n"
      "v_exp_f32 v51, v51\n"
      "v_smfmac_f32_32x32x32_bf16 v[0:15], v[84:87], v[64:71], v95\n"
      "ds_read_b128 v[84:87], v94 offset:3392\n"
      "v_exp_f32 v52, v52\n"
      "v_exp_f32 v53, v53\n"
      "v_exp_f32 v54, v54\n"
      "v_exp_f32 v55, v55\n"
      "v_mfma_f32_32x32x8_bf16 v[32:47], v[76:77], v[78:79], v[32:47]\n"
      "ds_read_b64 v[76:77], v93 offset:2688\n"
      "v_exp_f32 v64, v56\n"
      "v_exp_f32 v65, v57\n"
      "v_exp_f32 v66, v58\n"
      "v_exp_f32 v67, v59\n"
      "s_waitcnt lgkmcnt(3)\n"
      "v_smfmac_f32_32x32x32_bf16 v[0:15], v[80:83], v[48:55], v95\n"
      "ds_read_b128 v[80:83], v94 offset:2688\n"
      "v_exp_f32 v68, v60\n"
      "v_exp_f32 v69, v61\n"
      "v_exp_f32 v70, v62\n"
      "v_exp_f32 v71, v63\n"
      "s_setprio 2\n"
      "s_waitcnt lgkmcnt(1)\n"
      "v_mfma_f32_32x32x16_bf16 v[48:63], v[72:75], v[88:91], 0\n"
      "ds_read_b128 v[72:75], v92 offset:5376\n"
      "v_exp_f32 v32, v32\n"
      "v_exp_f32 v33, v33\n"
      "v_exp_f32 v34, v34\n"
      "v_exp_f32 v35, v35\n"
      "v_smfmac_f32_32x32x32_bf16 v[0:15], v[84:87], v[64:71], v95\n"
      "ds_read_b128 v[84:87], v94 offset:6080\n"
      "v_exp_f32 v36, v36\n"
      "v_exp_f32 v37, v37\n"
      "v_exp_f32 v38, v38\n"
      "v_exp_f32 v39, v39\n"
      "v_mfma_f32_32x32x8_bf16 v[48:63], v[76:77], v[78:79], v[48:63]\n"
      "ds_read_b64 v[76:77], v93 offset:5376\n"
      "v_exp_f32 v64, v40\n"
      "v_exp_f32 v65, v41\n"
      "v_exp_f32 v66, v42\n"
      "v_exp_f32 v67, v43\n"
      "s_waitcnt lgkmcnt(3)\n"
      "v_smfmac_f32_32x32x32_bf16 v[0:15], v[80:83], v[32:39], v95\n"
      "ds_read_b128 v[80:83], v94 offset:5376\n"
      "v_exp_f32 v68, v44\n"
      "v_exp_f32 v69, v45\n"
      "v_exp_f32 v70, v46\n"
      "v_exp_f32 v71, v47\n"
      "s_waitcnt lgkmcnt(1)\n"
      "v_mfma_f32_32x32x16_bf16 v[32:47], v[72:75], v[88:91], 0\n"
      "ds_read_b128 v[72:75], v92 offset:8064\n"
      "v_exp_f32 v48, v48\n"
      "v_exp_f32 v49, v49\n"
      "v_exp_f32 v50, v50\n"
      "v_exp_f32 v51, v51\n"
      "v_smfmac_f32_32x32x32_bf16 v[0:15], v[84:87], v[64:71], v95\n"
      "ds_read_b128 v[84:87], v94 offset:8768\n"
      "v_exp_f32 v52, v52\n"
      "v_exp_f32 v53, v53\n"
      "v_exp_f32 v54, v54\n"
      "v_exp_f32 v55, v55\n"
      "v_mfma_f32_32x32x8_bf16 v[32:47], v[76:77], v[78:79], v[32:47]\n"
      "ds_read_b64 v[76:77], v93 offset:8064\n"
      "v_exp_f32 v64, v56\n"
      "v_exp_f32 v65, v57\n"
      "v_exp_f32 v66, v58\n"
      "v_exp_f32 v67, v59\n"
      "s_waitcnt lgkmcnt(3)\n"
      "v_smfmac_f32_32x32x32_bf16 v[0:15], v[80:83], v[48:55], v95\n"
      "ds_read_b128 v[80:83], v94 offset:8064\n"
      "v_exp_f32 v68, v60\n"
      "v_exp_f32 v69, v61\n"
      "v_exp_f32 v70, v62\n"
      "v_exp_f32 v71, v63\n"
      "s_setprio 1\n"
      "s_waitcnt lgkmcnt(1)\n"
      "v_mfma_f32_32x32x16_bf16 v[48:63], v[72:75], v[88:91], 0\n"
      "ds_read_b128 v[72:75], v92 offset:10752\n"
      "v_exp_f32 v32, v32\n"
      "v_exp_f32 v33, v33\n"
      "v_exp_f32 v34, v34\n"
      "v_exp_f32 v35, v35\n"
      "v_smfmac_f32_32x32x32_bf16 v[0:15], v[84:87], v[64:71], v95\n"
      "ds_read_b128 v[84:87], v94 offset:11456\n"
      "v_exp_f32 v36, v36\n"
      "v_exp_f32 v37, v37\n"
      "v_exp_f32 v38, v38\n"
      "v_exp_f32 v39, v39\n"
      "v_mfma_f32_32x32x8_bf16 v[48:63], v[76:77], v[78:79], v[48:63]\n"
      "ds_read_b64 v[76:77], v93 offset:10752\n"
      "v_exp_f32 v64, v40\n"
      "v_exp_f32 v65, v41\n"
      "v_exp_f32 v66, v42\n"
      "v_exp_f32 v67, v43\n"
      "s_waitcnt lgkmcnt(3)\n"
      "v_smfmac_f32_32x32x32_bf16 v[0:15], v[80:83], v[32:39], v95\n"
      "ds_read_b128 v[80:83], v94 offset:10752\n"
      "v_exp_f32 v68, v44\n"
      "v_exp_f32 v69, v45\n"
      "v_exp_f32 v70, v46\n"
      "v_exp_f32 v71, v47\n"
      "s_waitcnt lgkmcnt(1)\n"
      "v_mfma_f32_32x32x16_bf16 v[32:47], v[72:75], v[88:91], 0\n"
      "ds_read_b128 v[72:75], v92 offset:13440\n"
      "v_exp_f32 v48, v48\n"
      "v_exp_f32 v49, v49\n"
      "v_exp_f32 v50, v50\n"
      "v_exp_f32 v51, v51\n"
      "v_smfmac_f32_32x32x32_bf16 v[0:15], v[84:87], v[64:71], v95\n"
      "ds_read_b128 v[84:87], v94 offset:14144\n"
      "v_exp_f32 v52, v52\n"
      "v_exp_f32 v53, v53\n"
      "v_exp_f32 v54, v54\n"
      "v_exp_f32 v55, v55\n"
      "v_mfma_f32_32x32x8_bf16 v[32:47], v[76:77], v[78:79], v[32:47]\n"
      "ds_read_b64 v[76:77], v93 offset:13440\n"
      "v_exp_f32 v64, v56\n"
      "v_exp_f32 v65, v57\n"
      "v_exp_f32 v66, v58\n"
      "v_exp_f32 v67, v59\n"
      "s_waitcnt lgkmcnt(3)\n"
      "v_smfmac_f32_32x32x32_bf16 v[0:15], v[80:83], v[48:55], v95\n"
      "ds_read_b128 v[80:83], v94 offset:13440\n"
      "v_exp_f32 v68, v60\n"
      "v_exp_f32 v69, v61\n"
      "v_exp_f32 v70, v62\n"
      "v_exp_f32 v71, v63\n"
      "s_setprio 0\n"
      "s_waitcnt lgkmcnt(1)\n"
      "v_mfma_f32_32x32x16_bf16 v[48:63], v[72:75], v[88:91], 0\n"
      "ds_read_b128 v[72:75], v92 offset:16128\n"
      "v_exp_f32 v32, v32\n"
      "v_exp_f32 v33, v33\n"
      "v_exp_f32 v34, v34\n"
      "v_exp_f32 v35, v35\n"
      "v_smfmac_f32_32x32x32_bf16 v[0:15], v[84:87], v[64:71], v95\n"
      "ds_read_b128 v[84:87], v94 offset:16832\n"
      "v_exp_f32 v36, v36\n"
      "v_exp_f32 v37, v37\n"
      "v_exp_f32 v38, v38\n"
      "v_exp_f32 v39, v39\n"
      "v_mfma_f32_32x32x8_bf16 v[48:63], v[76:77], v[78:79], v[48:63]\n"
      "ds_read_b64 v[76:77], v93 offset:16128\n"
      "v_exp_f32 v64, v40\n"
      "v_exp_f32 v65, v41\n"
      "v_exp_f32 v66, v42\n"
      "v_exp_f32 v67, v43\n"
      "s_waitcnt lgkmcnt(3)\n"
      "v_smfmac_f32_32x32x32_bf16 v[0:15], v[80:83], v[32:39], v95\n"
      "ds_read_b128 v[80:83], v94 offset:16128\n"
      "v_exp_f32 v68, v44\n"
      "v_exp_f32 v69, v45\n"
      "v_exp_f32 v70, v46\n"
      "v_exp_f32 v71, v47\n"
      "s_waitcnt lgkmcnt(1)\n"
      "v_mfma_f32_32x32x16_bf16 v[32:47], v[72:75], v[88:91], 0\n"
      "ds_read_b128 v[72:75], v92 offset:18816\n"
      "v_exp_f32 v48, v48\n"
      "v_exp_f32 v49, v49\n"
      "v_exp_f32 v50, v50\n"
      "v_exp_f32 v51, v51\n"
      "v_smfmac_f32_32x32x32_bf16 v[0:15], v[84:87], v[64:71], v95\n"
      "ds_read_b128 v[84:87], v94 offset:19520\n"
      "v_exp_f32 v52, v52\n"
      "v_exp_f32 v53, v53\n"
      "v_exp_f32 v54, v54\n"
      "v_exp_f32 v55, v55\n"
      "v_mfma_f32_32x32x8_bf16 v[32:47], v[76:77], v[78:79], v[32:47]\n"
      "ds_read_b64 v[76:77], v93 offset:18816\n"
      "v_exp_f32 v64, v56\n"
      "v_exp_f32 v65, v57\n"
      "v_exp_f32 v66, v58\n"
      "v_exp_f32 v67, v59\n"
      "s_waitcnt lgkmcnt(3)\n"
      "v_smfmac_f32_32x32x32_bf16 v[0:15], v[80:83], v[48:55], v95\n"
      "ds_read_b128 v[80:83], v94 offset:18816\n"
      "v_exp_f32 v68, v60\n"
      "v_exp_f32 v69, v61\n"
      "v_exp_f32 v70, v62\n"
      "v_exp_f32 v71, v63\n"
      "s_cmp_eq_u32 %[j], 2\n"
      "s_cbranch_scc1 5f\n"
      "6:\n"
      "s_setprio 3\n"
      "s_waitcnt lgkmcnt(1)\n"
      "v_mfma_f32_32x32x16_bf16 v[48:63], v[72:75], v[88:91], 0\n"
      "ds_read_b128 v[72:75], v92 offset:21504\n"
      "v_exp_f32 v32, v32\n"
      "v_exp_f32 v33, v33\n"
      "v_exp_f32 v34, v34\n"
      "v_exp_f32 v35, v35\n"
      "v_smfmac_f32_32x32x32_bf16 v[0:15], v[84:87], v[64:71], v95\n"
      "ds_read_b128 v[84:87], v94 offset:22208\n"
      "v_exp_f32 v36, v36\n"
      "v_exp_f32 v37, v37\n"
      "v_exp_f32 v38, v38\n"
      "v_exp_f32 v39, v39\n"
      "v_mfma_f32_32x32x8_bf16 v[48:63], v[76:77], v[78:79], v[48:63]\n"
      "ds_read_b64 v[76:77], v93 offset:21504\n"
      "v_exp_f32 v64, v40\n"
      "v_exp_f32 v65, v41\n"
      "v_exp_f32 v66, v42\n"
      "v_exp_f32 v67, v43\n"
      "s_waitcnt lgkmcnt(3)\n"
      "v_smfmac_f32_32x32x32_bf16 v[0:15], v[80:83], v[32:39], v95\n"
      "ds_read_b128 v[80:83], v94 offset:21504\n"
      "v_exp_f32 v68, v44\n"
      "v_exp_f32 v69, v45\n"
      "v_exp_f32 v70, v46\n"
      "v_exp_f32 v71, v47\n"
      "s_waitcnt lgkmcnt(1)\n"
      "v_mfma_f32_32x32x16_bf16 v[32:47], v[72:75], v[88:91], 0\n"
      "ds_read_b128 v[72:75], v92 offset:24192\n"
      "v_exp_f32 v48, v48\n"
      "v_exp_f32 v49, v49\n"
      "v_exp_f32 v50, v50\n"
      "v_exp_f32 v51, v51\n"
      "v_smfmac_f32_32x32x32_bf16 v[0:15], v[84:87], v[64:71], v95\n"
      "ds_read_b128 v[84:87], v94 offset:24896\n"
      "v_exp_f32 v52, v52\n"
      "v_exp_f32 v53, v53\n"
      "v_exp_f32 v54, v54\n"
      "v_exp_f32 v55, v55\n"
      "v_mfma_f32_32x32x8_bf16 v[32:47], v[76:77], v[78:79], v[32:47]\n"
      "ds_read_b64 v[76:77], v93 offset:24192\n"
      "v_exp_f32 v64, v56\n"
      "v_exp_f32 v65, v57\n"
      "v_exp_f32 v66, v58\n"
      "v_exp_f32 v67, v59\n"
      "s_waitcnt lgkmcnt(3)\n"
      "v_smfmac_f32_32x32x32_bf16 v[0:15], v[80:83], v[48:55], v95\n"
      "ds_read_b128 v[80:83], v94 offset:24192\n"
      "v_exp_f32 v68, v60\n"
      "v_exp_f32 v69, v61\n"
      "v_exp_f32 v70, v62\n"
      "v_exp_f32 v71, v63\n"
      "s_setprio 2\n"
      "s_waitcnt lgkmcnt(1)\n"
      "v_mfma_f32_32x32x16_bf16 v[48:63], v[72:75], v[88:91], 0\n"
      "ds_read_b128 v[72:75], v92 offset:26880\n"
      "v_exp_f32 v32, v32\n"
      "v_exp_f32 v33, v33\n"
      "v_exp_f32 v34, v34\n"
      "v_exp_f32 v35, v35\n"
      "v_smfmac_f32_32x32x32_bf16 v[0:15], v[84:87], v[64:71], v95\n"
      "ds_read_b128 v[84:87], v94 offset:27584\n"
      "v_exp_f32 v36, v36\n"
      "v_exp_f32 v37, v37\n"
      "v_exp_f32 v38, v38\n"
      "v_exp_f32 v39, v39\n"
      "v_mfma_f32_32x32x8_bf16 v[48:63], v[76:77], v[78:79], v[48:63]\n"
      "ds_read_b64 v[76:77], v93 offset:26880\n"
      "v_exp_f32 v64, v40\n"
      "v_exp_f32 v65, v41\n"
      "v_exp_f32 v66, v42\n"
      "v_exp_f32 v67, v43\n"
      "s_waitcnt lgkmcnt(3)\n"
      "v_smfmac_f32_32x32x32_bf16 v[0:15], v[80:83], v[32:39], v95\n"
      "ds_read_b128 v[80:83], v94 offset:26880\n"
      "v_exp_f32 v68, v44\n"
      "v_exp_f32 v69, v45\n"
      "v_exp_f32 v70, v46\n"
      "v_exp_f32 v71, v47\n"
      "s_waitcnt lgkmcnt(1)\n"
      "v_mfma_f32_32x32x16_bf16 v[32:47], v[72:75], v[88:91], 0\n"
      "ds_read_b128 v[72:75], v92 offset:29568\n"
      "v_exp_f32 v48, v48\n"
      "v_exp_f32 v49, v49\n"
      "v_exp_f32 v50, v50\n"
      "v_exp_f32 v51, v51\n"
      "v_smfmac_f32_32x32x32_bf16 v[0:15], v[84:87], v[64:71], v95\n"
      "ds_read_b128 v[84:87], v94 offset:30272\n"
      "v_exp_f32 v52, v52\n"
      "v_exp_f32 v53, v53\n"
      "v_exp_f32 v54, v54\n"
      "v_exp_f32 v55, v55\n"
      "v_mfma_f32_32x32x8_bf16 v[32:47], v[76:77], v[78:79], v[32:47]\n"
      "ds_read_b64 v[76:77], v93 offset:29568\n"
      "v_exp_f32 v64, v56\n"
      "v_exp_f32 v65, v57\n"
      "v_exp_f32 v66, v58\n"
      "v_exp_f32 v67, v59\n"
      "s_waitcnt lgkmcnt(3)\n"
      "v_smfmac_f32_32x32x32_bf16 v[0:15], v[80:83], v[48:55], v95\n"
      "ds_read_b128 v[80:83], v94 offset:29568\n"
      "v_exp_f32 v68, v60\n"
      "v_exp_f32 v69, v61\n"
      "v_exp_f32 v70, v62\n"
      "v_exp_f32 v71, v63\n"
      "s_setprio 1\n"
      "s_waitcnt lgkmcnt(1)\n"
      "v_mfma_f32_32x32x16_bf16 v[48:63], v[72:75], v[88:91], 0\n"
      "ds_read_b128 v[72:75], v92 offset:32256\n"
      "v_exp_f32 v32, v32\n"
      "v_exp_f32 v33, v33\n"
      "v_exp_f32 v34, v34\n"
      "v_exp_f32 v35, v35\n"
      "v_smfmac_f32_32x32x32_bf16 v[0:15], v[84:87], v[64:71], v95\n"
      "ds_read_b128 v[84:87], v94 offset:32960\n"
      "v_exp_f32 v36, v36\n"
      "v_exp_f32 v37, v37\n"
      "v_exp_f32 v38, v38\n"
      "v_exp_f32 v39, v39\n"
      "v_mfma_f32_32x32x8_bf16 v[48:63], v[76:77], v[78:79], v[48:63]\n"
      "ds_read_b64 v[76:77], v93 offset:32256\n"
      "v_exp_f32 v64, v40\n"
      "v_exp_f32 v65, v41\n"
      "v_exp_f32 v66, v42\n"
      "v_exp_f32 v67, v43\n"
      "s_waitcnt lgkmcnt(3)\n"
      "v_smfmac_f32_32x32x32_bf16 v[0:15], v[80:83], v[32:39], v95\n"
      "ds_read_b128 v[80:83], v94 offset:32256\n"
      "v_exp_f32 v68, v44\n"
      "v_exp_f32 v69, v45\n"
      "v_exp_f32 v70, v46\n"
      "v_exp_f32 v71, v47\n"
      "s_waitcnt lgkmcnt(1)\n"
      "v_mfma_f32_32x32x16_bf16 v[32:47], v[72:75], v[88:91], 0\n"
      "ds_read_b128 v[72:75], v92 offset:34944\n"
      "v_exp_f32 v48, v48\n"
      "v_exp_f32 v49, v49\n"
      "v_exp_f32 v50, v50\n"
      "v_exp_f32 v51, v51\n"
      "v_smfmac_f32_32x32x32_bf16 v[0:15], v[84:87], v[64:71], v95\n"
      "ds_read_b128 v[84:87], v94 offset:35648\n"
      "v_exp_f32 v52, v52\n"
      "v_exp_f32 v53, v53\n"
      "v_exp_f32 v54, v54\n"
      "v_exp_f32 v55, v55\n"
      "v_mfma_f32_32x32x8_bf16 v[32:47], v[76:77], v[78:79], v[32:47]\n"
      "ds_read_b64 v[76:77], v93 offset:34944\n"
      "v_exp_f32 v64, v56\n"
      "v_exp_f32 v65, v57\n"
      "v_exp_f32 v66, v58\n"
      "v_exp_f32 v67, v59\n"
      "s_waitcnt lgkmcnt(3)\n"
      "v_smfmac_f32_32x32x32_bf16 v[0:15], v[80:83], v[48:55], v95\n"
      "ds_read_b128 v[80:83], v94 offset:34944\n"
      "v_exp_f32 v68, v60\n"
      "v_exp_f32 v69, v61\n"
      "v_exp_f32 v70, v62\n"
      "v_exp_f32 v71, v63\n"
      "s_setprio 0\n"
      "s_waitcnt lgkmcnt(1)\n"
      "v_mfma_f32_32x32x16_bf16 v[48:63], v[72:75], v[88:91], 0\n"
      "ds_read_b128 v[72:75], v92 offset:37632\n"
      "v_exp_f32 v32, v32\n"
      "v_exp_f32 v33, v33\n"
      "v_exp_f32 v34, v34\n"
      "v_exp_f32 v35, v35\n"
      "v_smfmac_f32_32x32x32_bf16 v[0:15], v[84:87], v[64:71], v95\n"
      "ds_read_b128 v[84:87], v94 offset:38336\n"
      "v_exp_f32 v36, v36\n"
      "v_exp_f32 v37, v37\n"
      "v_exp_f32 v38, v38\n"
      "v_exp_f32 v39, v39\n"
      "v_mfma_f32_32x32x8_bf16 v[48:63], v[76:77], v[78:79], v[48:63]\n"
      "ds_read_b64 v[76:77], v93 offset:37632\n"
      "v_exp_f32 v64, v40\n"
      "v_exp_f32 v65, v41\n"
      "v_exp_f32 v66, v42\n"
      "v_exp_f32 v67, v43\n"
      "s_waitcnt lgkmcnt(3)\n"
      "v_smfmac_f32_32x32x32_bf16 v[0:15], v[80:83], v[32:39], v95\n"
      "ds_read_b32 %[rdy], %[ardy]\n"
      "ds_read_b128 v[80:83], v94 offset:37632\n"
      "v_exp_f32 v68, v44\n"
      "v_exp_f32 v69, v45\n"
      "v_exp_f32 v70, v46\n"
      "v_exp_f32 v71, v47\n"
      "s_waitcnt lgkmcnt(1)\n"
      "v_mfma_f32_32x32x16_bf16 v[32:47], v[72:75], v[88:91], 0\n"
      "ds_read_b128 v[72:75], v92 offset:40320\n"
      "v_exp_f32 v48, v48\n"
      "v_exp_f32 v49, v49\n"
      "v_exp_f32 v50, v50\n"
      "v_exp_f32 v51, v51\n"
      "v_smfmac_f32_32x32x32_bf16 v[0:15], v[84:87], v[64:71], v95\n"
      "ds_read_b128 v[84:87], v94 offset:41024\n"
      "v_exp_f32 v52, v52\n"
      "v_exp_f32 v53, v53\n"
      "v_exp_f32 v54, v54\n"
      "v_exp_f32 v55, v55\n"
      "v_mfma_f32_32x32x8_bf16 v[32:47], v[76:77], v[78:79], v[32:47]\n"
      "ds_read_b64 v[76:77], v93 offset:40320\n"
      "v_exp_f32 v64, v56\n"
      "v_exp_f32 v65, v57\n"
      "v_exp_f32 v66, v58\n"
      "v_exp_f32 v67, v59\n"
      "s_waitcnt lgkmcnt(3)\n"
      "v_smfmac_f32_32x32x32_bf16 v[0:15], v[80:83], v[48:55], v95\n"
      "ds_read_b128 v[80:83], v94 offset:40320\n"
      "v_mov_b32 %[dnv], %[j]\n"
      "s_add_u32 %[j], %[j], 2\n"
      "s_min_u32 %[j], %[j], 9\n"
      "s_cmp_eq_u32 %[j], 8\n"
      "s_cselect_b32 %[n], 4294881280, %[hc]\n"
      "v_add_u32 v92, %[n], v92\n"
      "v_add_u32 v93, %[n], v93\n"
      "v_add_u32 v94, %[n], v94\n"
      "v_cmp_ge_u32 vcc, %[j], %[rdy]\n"
      "v_exp_f32 v68, v60\n"
      "v_exp_f32 v69, v61\n"
      "v_exp_f32 v70, v62\n"
      "v_exp_f32 v71, v63\n"
      "s_cbranch_vccnz 3f\n"
      "2:\n"
      "s_cmp_lt_u32 %[j], 9\n"
      "s_cbranch_scc1 0b\n"
      "s_branch 4f\n"
      "3:\n"
      "s_mov_b32 %[n], 0\n"
      "s_waitcnt lgkmcnt(0)\n"
      "ds_write_b32 %[adn], %[dnv]\n"
      "1:\n"
      "ds_read_b32 %[rdy], %[ardy]\n"
      "s_add_u32 %[n], %[n], 1\n"
      "s_waitcnt lgkmcnt(0)\n"
      "v_cmp_ge_u32 vcc, %[j], %[rdy]\n"
      "s_cbranch_vccz 2b\n"
      "s_cmp_gt_u32 %[n], %[cap]\n"
      "s_cbranch_scc1 2b\n"
      "s_sleep 1\n"
      "s_branch 1b\n"
      "5:\n"
      "s_mov_b32 %[n], 0\n"
      "s_waitcnt lgkmcnt(0)\n"
      "ds_write_b32 %[adn], %[dnv]\n"
      "1:\n"
      "ds_read_b32 %[rdy], %[ardy]\n"
      "s_add_u32 %[n], %[n], 1\n"
      "s_waitcnt lgkmcnt(0)\n"
      "v_cmp_ge_u32 vcc, %[j], %[rdy]\n"
      "s_cbranch_vccz 6b\n"
      "s_cmp_gt_u32 %[n], %[cap]\n"
      "s_cbranch_scc1 6b\n"
      "s_sleep 1\n"
      "s_branch 1b\n"
      "4:\n"
      "s_waitcnt lgkmcnt(0)\n"
      "v_smfmac_f32_32x32x32_bf16 v[0:15], v[84:87], v[64:71], v95\n"
      "s_nop 7\n"
      "s_nop 3\n"
        : "+{v[0:15]}"(o), "+{v[32:47]}"(s0), "+{v[64:71]}"(p1), "+{v[72:75]}"(kf1), "+{v[76:77]}"(kf2), "+{v[80:83]}"(vf0), "+{v[84:87]}"(vf1),
          "+{v92}"(ak), "+{v93}"(ak2), "+{v94}"(av), [rdy] "=&v"(rdy), [dnv] "=&v"(dnv), [j] "=&s"(sj), [n] "=&s"(sn)
        : "{v[88:91]}"(q1), "{v[78:79]}"(q2m), "{v95}"(SPIDX), [ardy] "v"(ardy), [adn] "v"(LM + M_DN + w * 4), [hc] "s"(2 * HC), [cap] "s"(SPIN_CAP << 2)
        : "v48", "v49", "v50", "v51", "v52", "v53", "v54", "v55", "v56", "v57", "v58", "v59", "v60", "v61", "v62", "v63", "vcc", "scc", "memory");
    }

    float l = upper_half(o[8]);
    {
      float am = h ? 0.f : (fabsf(o[8]) + fabsf(o[9])) + (fabsf(o[10]) + fabsf(o[11]));
#pragma unroll
      for (int i = 0; i < 8; ++i) am += fabsf(o[i]);
      const bool ok = (l >= 0x1p-60f) && (l <= 0x1p60f) && (am <= 0x1p126f);
      if (__builtin_expect(!__all(ok), 0)) {
        float m = -INFINITY;
        for (int t = 0; t < 64; ++t) {
          const char* rk = kvg + t * REC;
          f32x16 s = mfma32(__builtin_bit_cast(bf16x8, ld128_dev(rk, l16)), q1, zero); s = mfma32k8(__builtin_bit_cast(bf16x4, ld64_dev(rk, ok2)), q2, s);
          m = fmaxf(m, tile_max(s));
        }
        m = max_halves(m);
        f32x16 nm;
#pragma unroll
        for (int i = 0; i < 16; ++i) nm[i] = -m;
        o = zero;
        for (int t = 0; t < 64; ++t) {
          const char* rk = kvg + ((t - 2) & 63) * REC;
          f32x16 s = mfma32(__builtin_bit_cast(bf16x8, ld128_dev(rk, l16)), q1, nm); s = mfma32k8(__builtin_bit_cast(bf16x4, ld64_dev(rk, ok2)), q2, s);
#pragma unroll
          for (int i = 0; i < 16; ++i) s[i] = __builtin_amdgcn_exp2f(s[i]);
          o = pv32(__builtin_bit_cast(bf16x8, ld128_dev(kvg + ((t - 1) & 63) * REC, ov)), __builtin_shufflevector(s, s, 0, 1, 2, 3, 4, 5, 6, 7), o);
          o = pv32(__builtin_bit_cast(bf16x8, ld128_dev(kvg + t * REC + (R_V1 - R_V0), ov)), __builtin_shufflevector(s, s, 8, 9, 10, 11, 12, 13, 14, 15), o);
        }
        l = upper_half(o[8]);
      }
    }
    {
      const float rl = __builtin_amdgcn_rcpf(l);
      auto pw = [&](int i) { const unsigned v = pk2(o[i] * rl, o[i + 1] * rl); return v < 0xfffffffeu ? v : 0xfffffffeu; };
      st128_dev(og, oa1, (u32x4){pw(0), pw(2), pw(4), pw(6)});
      if (h == 0) st64_dev(og, oa2, (u32x2){pw(8), pw(10)});
    }

    __builtin_amdgcn_s_sleep(TDELAY); if (b >= 192) __builtin_amdgcn_s_sleep(TDELAY2);

    unsigned outo[2], exr[2], gad[2];
#pragma unroll
    for (int n = 0; n < 2; ++n) {
      const unsigned u = (n && w < 2 ? 512 : 0) + w * 64 + lane, pos = u / 10, cu = u - pos * 10, row = wrow(pos);
      outo[n] = (rt * 64 + row) * 1280 + cg * 160 + cu * 16; exr[n] = L + row * EXROW + cu * 32; gad[n] = LM + M_G + row * 4;
      PIN(outo[n]); PIN(exr[n]); PIN(gad[n]);
    }
    unsigned exw = L + kh * EXH + (tt * 16 + c) * EXROW + g * 16;
    unsigned am = L + L_WO + kh * 51200 + wpos((g >> 1) * 20 + 4 * (g & 1) + q4) * 160 + p4 * 8;
    unsigned ar = L + L_WO + kh * 51200 + wpos((g >> 1) * 80 + (g & 1) * 20 + 16 + q4) * 160 + p4 * 8, arem = g * 256 + c * 8;
    PIN(exw); PIN(am); PIN(ar); PIN(arem);
    f32x4 acc[5];
    {
      const char* osrc = og + (tt * 20 + kh * 10) * 1024;
      unsigned spin; unsigned long long ret;
      asm volatile(
      "ds_read_b64_tr_b16 v[60:61], v142 offset:0\n"
      "ds_read_b64_tr_b16 v[62:63], v142 offset:1280\n"
      "ds_read_b64_tr_b16 v[64:65], v142 offset:32\n"
      "ds_read_b64_tr_b16 v[66:67], v142 offset:1312\n"
      "ds_read_b64_tr_b16 v[68:69], v142 offset:64\n"
      "ds_read_b64_tr_b16 v[70:71], v142 offset:1344\n"
      "ds_read_b64_tr_b16 v[72:73], v142 offset:96\n"
      "ds_read_b64_tr_b16 v[74:75], v142 offset:1376\n"
      "ds_read_b64_tr_b16 v[76:77], v142 offset:128\n"
      "ds_read_b64_tr_b16 v[78:79], v142 offset:1408\n"
      "ds_read_b64_tr_b16 v[80:81], v142 offset:6400\n"
      "ds_read_b64_tr_b16 v[82:83], v142 offset:7680\n"
      "ds_read_b64_tr_b16 v[84:85], v142 offset:6432\n"
      "ds_read_b64_tr_b16 v[86:87], v142 offset:7712\n"
      "ds_read_b64_tr_b16 v[88:89], v142 offset:6464\n"
      "ds_read_b64_tr_b16 v[90:91], v142 offset:7744\n"
      "ds_read_b64_tr_b16 v[92:93], v142 offset:6496\n"
      "ds_read_b64_tr_b16 v[94:95], v142 offset:7776\n"
      "ds_read_b64_tr_b16 v[96:97], v142 offset:6528\n"
      "ds_read_b64_tr_b16 v[98:99], v142 offset:7808\n"
      "ds_read_b64_tr_b16 v[100:101], v142 offset:12800\n"
      "ds_read_b64_tr_b16 v[102:103], v142 offset:14080\n"
      "ds_read_b64_tr_b16 v[104:105], v142 offset:12832\n"
      "ds_read_b64_tr_b16 v[106:107], v142 offset:14112\n"
      "ds_read_b64_tr_b16 v[108:109], v142 offset:12864\n"
      "ds_read_b64_tr_b16 v[110:111], v142 offset:14144\n"
      "ds_read_b64_tr_b16 v[112:113], v142 offset:12896\n"
      "ds_read_b64_tr_b16 v[114:115], v142 offset:14176\n"
      "ds_read_b64_tr_b16 v[116:117], v142 offset:12928\n"
      "ds_read_b64_tr_b16 v[118:119], v142 offset:14208\n"
      "ds_read_b64_tr_b16 v[120:121], v142 offset:19200\n"
      "ds_read_b64_tr_b16 v[122:123], v142 offset:20480\n"
      "ds_read_b64_tr_b16 v[124:125], v142 offset:19232\n"
      "ds_read_b64_tr_b16 v[126:127], v142 offset:20512\n"
      "ds_read_b64_tr_b16 v[128:129], v142 offset:19264\n"
      "ds_read_b64_tr_b16 v[130:131], v142 offset:20544\n"
      "ds_read_b64_tr_b16 v[132:133], v142 offset:19296\n"
      "ds_read_b64_tr_b16 v[134:135], v142 offset:20576\n"
      "ds_read_b64_tr_b16 v[136:137], v142 offset:19328\n"
      "ds_read_b64_tr_b16 v[138:139], v142 offset:20608\n"
      "s_mov_b32 %[spin], 0\n"
      "s_barrier\n"
      "s_setprio 3\n"
      "global_load_dwordx4 v[20:23], v141, %[ob0] offset:0 sc1\n"
      "global_load_dwordx4 v[24:27], v141, %[ob0] offset:1024 sc1\n"
      "s_setprio 2\n"
      "global_load_dwordx4 v[28:31], v141, %[ob0] offset:2048 sc1\n"
      "global_load_dwordx4 v[32:35], v141, %[ob0] offset:3072 sc1\n"
      "global_load_dwordx4 v[36:39], v141, %[ob1] offset:0 sc1\n"
      "s_setprio 3\n"
      "s_getpc_b64 %[ret]\n"
      "s_waitcnt vmcnt(4)\n"
      "v_max3_u32 v140, v20, v21, v22\n"
      "v_max_u32 v140, v140, v23\n"
      "v_cmp_eq_u32 vcc, -1, v140\n"
      "s_cbranch_vccnz 50f\n"
      "s_waitcnt lgkmcnt(15)\n"
      "v_mfma_f32_16x16x32_bf16 v[0:3], v[60:63], v[20:23], 0\n"
      "global_load_dwordx4 v[40:43], v141, %[ob1] offset:1024 sc1\n"
      "ds_read_b64_tr_b16 v[60:61], v142 offset:25600\n"
      "ds_read_b64_tr_b16 v[62:63], v142 offset:26880\n"
      "v_mfma_f32_16x16x32_bf16 v[4:7], v[64:67], v[20:23], 0\n"
      "ds_read_b64_tr_b16 v[64:65], v142 offset:25632\n"
      "ds_read_b64_tr_b16 v[66:67], v142 offset:26912\n"
      "v_mfma_f32_16x16x32_bf16 v[8:11], v[68:71], v[20:23], 0\n"
      "ds_read_b64_tr_b16 v[68:69], v142 offset:25664\n"
      "ds_read_b64_tr_b16 v[70:71], v142 offset:26944\n"
      "v_mfma_f32_16x16x32_bf16 v[12:15], v[72:75], v[20:23], 0\n"
      "ds_read_b64_tr_b16 v[72:73], v142 offset:25696\n"
      "ds_read_b64_tr_b16 v[74:75], v142 offset:26976\n"
      "v_mfma_f32_16x16x32_bf16 v[16:19], v[76:79], v[20:23], 0\n"
      "ds_read_b64_tr_b16 v[76:77], v142 offset:25728\n"
      "ds_read_b64_tr_b16 v[78:79], v142 offset:27008\n"
      "s_getpc_b64 %[ret]\n"
      "s_waitcnt vmcnt(4)\n"
      "v_max3_u32 v140, v24, v25, v26\n"
      "v_max_u32 v140, v140, v27\n"
      "v_cmp_eq_u32 vcc, -1, v140\n"
      "s_cbranch_vccnz 51f\n"
      "s_waitcnt lgkmcnt(15)\n"
      "v_mfma_f32_16x16x32_bf16 v[0:3], v[80:83], v[24:27], v[0:3]\n"
      "global_load_dwordx4 v[44:47], v141, %[ob1] offset:2048 sc1\n"
      "ds_read_b64_tr_b16 v[80:81], v142 offset:32000\n"
      "ds_read_b64_tr_b16 v[82:83], v142 offset:33280\n"
      "v_mfma_f32_16x16x32_bf16 v[4:7], v[84:87], v[24:27], v[4:7]\n"
      "ds_read_b64_tr_b16 v[84:85], v142 offset:32032\n"
      "ds_read_b64_tr_b16 v[86:87], v142 offset:33312\n"
      "v_mfma_f32_16x16x32_bf16 v[8:11], v[88:91], v[24:27], v[8:11]\n"
      "ds_read_b64_tr_b16 v[88:89], v142 offset:32064\n"
      "ds_read_b64_tr_b16 v[90:91], v142 offset:33344\n"
      "v_mfma_f32_16x16x32_bf16 v[12:15], v[92:95], v[24:27], v[12:15]\n"
      "ds_read_b64_tr_b16 v[92:93], v142 offset:32096\n"
      "ds_read_b64_tr_b16 v[94:95], v142 offset:33376\n"
      "v_mfma_f32_16x16x32_bf16 v[16:19], v[96:99], v[24:27], v[16:19]\n"
      "ds_read_b64_tr_b16 v[96:97], v142 offset:32128\n"
      "ds_read_b64_tr_b16 v[98:99], v142 offset:33408\n"
      "s_getpc_b64 %[ret]\n"
      "s_waitcnt vmcnt(4)\n"
      "v_max3_u32 v140, v28, v29, v30\n"
      "v_max_u32 v140, v140, v31\n"
      "v_cmp_eq_u32 vcc, -1, v140\n"
      "s_cbranch_vccnz 52f\n"
      "s_waitcnt lgkmcnt(15)\n"
      "v_mfma_f32_16x16x32_bf16 v[0:3], v[100:103], v[28:31], v[0:3]\n"
      "global_load_dwordx4 v[48:51], v141, %[ob1] offset:3072 sc1\n"
      "ds_read_b64_tr_b16 v[100:101], v142 offset:38400\n"
      "ds_read_b64_tr_b16 v[102:103], v142 offset:39680\n"
      "v_mfma_f32_16x16x32_bf16 v[4:7], v[104:107], v[28:31], v[4:7]\n"
      "ds_read_b64_tr_b16 v[104:105], v142 offset:38432\n"
      "ds_read_b64_tr_b16 v[106:107], v142 offset:39712\n"
      "v_mfma_f32_16x16x32_bf16 v[8:11], v[108:111], v[28:31], v[8:11]\n"
      "ds_read_b64_tr_b16 v[108:109], v142 offset:38464\n"
      "ds_read_b64_tr_b16 v[110:111], v142 offset:39744\n"
      "v_mfma_f32_16x16x32_bf16 v[12:15], v[112:115], v[28:31], v[12:15]\n"
      "ds_read_b64_tr_b16 v[112:113], v142 offset:38496\n"
      "ds_read_b64_tr_b16 v[114:115], v142 offset:39776\n"
      "v_mfma_f32_16x16x32_bf16 v[16:19], v[116:119], v[28:31], v[16:19]\n"
      "ds_read_b64_tr_b16 v[116:117], v142 offset:38528\n"
      "ds_read_b64_tr_b16 v[118:119], v142 offset:39808\n"
      "s_setprio 2\n"
      "s_getpc_b64 %[ret]\n"
      "s_waitcnt vmcnt(4)\n"
      "v_max3_u32 v140, v32, v33, v34\n"
      "v_max_u32 v140, v140, v35\n"
      "v_cmp_eq_u32 vcc, -1, v140\n"
      "s_cbranch_vccnz 53f\n"
      "s_waitcnt lgkmcnt(15)\n"
      "v_mfma_f32_16x16x32_bf16 v[0:3], v[120:123], v[32:35], v[0:3]\n"
      "global_load_dwordx2 v[52:53], %[arem], %[ob2] offset:0 sc1\n"
      "global_load_dwordx2 v[54:55], %[arem], %[ob2] offset:128 sc1\n"
      "ds_read_b64_tr_b16 v[120:121], v142 offset:44800\n"
      "ds_read_b64_tr_b16 v[122:123], v142 offset:46080\n"
      "v_mfma_f32_16x16x32_bf16 v[4:7], v[124:127], v[32:35], v[4:7]\n"
      "ds_read_b64_tr_b16 v[124:125], v142 offset:44832\n"
      "ds_read_b64_tr_b16 v[126:127], v142 offset:46112\n"
      "v_mfma_f32_16x16x32_bf16 v[8:11], v[128:131], v[32:35], v[8:11]\n"
      "ds_read_b64_tr_b16 v[128:129], v142 offset:44864\n"
      "ds_read_b64_tr_b16 v[130:131], v142 offset:46144\n"
      "v_mfma_f32_16x16x32_bf16 v[12:15], v[132:135], v[32:35], v[12:15]\n"
      "ds_read_b64_tr_b16 v[132:133], v142 offset:44896\n"
      "ds_read_b64_tr_b16 v[134:135], v142 offset:46176\n"
      "v_mfma_f32_16x16x32_bf16 v[16:19], v[136:139], v[32:35], v[16:19]\n"
      "ds_read_b64_tr_b16 v[136:137], v142 offset:44928\n"
      "ds_read_b64_tr_b16 v[138:139], v142 offset:46208\n"
      "s_getpc_b64 %[ret]\n"
      "s_waitcnt vmcnt(5)\n"
      "v_max3_u32 v140, v36, v37, v38\n"
      "v_max_u32 v140, v140, v39\n"
      "v_cmp_eq_u32 vcc, -1, v140\n"
      "s_cbranch_vccnz 54f\n"
      "s_waitcnt lgkmcnt(15)\n"
      "v_mfma_f32_16x16x32_bf16 v[0:3], v[60:63], v[36:39], v[0:3]\n"
      "global_load_dwordx2 v[56:57], %[arem], %[ob2] offset:1024 sc1\n"
      "global_load_dwordx2 v[58:59], %[arem], %[ob2] offset:1152 sc1\n"
      "ds_read_b64_tr_b16 v[60:61], v143 offset:0\n"
      "ds_read_b64_tr_b16 v[62:63], v143 offset:6400\n"
      "v_mfma_f32_16x16x32_bf16 v[4:7], v[64:67], v[36:39], v[4:7]\n"
      "ds_read_b64_tr_b16 v[64:65], v143 offset:32\n"
      "ds_read_b64_tr_b16 v[66:67], v143 offset:6432\n"
      "v_mfma_f32_16x16x32_bf16 v[8:11], v[68:71], v[36:39], v[8:11]\n"
      "ds_read_b64_tr_b16 v[68:69], v143 offset:64\n"
      "ds_read_b64_tr_b16 v[70:71], v143 offset:6464\n"
      "v_mfma_f32_16x16x32_bf16 v[12:15], v[72:75], v[36:39], v[12:15]\n"
      "ds_read_b64_tr_b16 v[72:73], v143 offset:96\n"
      "ds_read_b64_tr_b16 v[74:75], v143 offset:6496\n"
      "v_mfma_f32_16x16x32_bf16 v[16:19], v[76:79], v[36:39], v[16:19]\n"
      "ds_read_b64_tr_b16 v[76:77], v143 offset:128\n"
      "ds_read_b64_tr_b16 v[78:79], v143 offset:6528\n"
      "s_getpc_b64 %[ret]\n"
      "s_waitcnt vmcnt(6)\n"
      "v_max3_u32 v140, v40, v41, v42\n"
      "v_max_u32 v140, v140, v43\n"
      "v_cmp_eq_u32 vcc, -1, v140\n"
      "s_cbranch_vccnz 55f\n"
      "s_waitcnt lgkmcnt(15)\n"
      "v_mfma_f32_16x16x32_bf16 v[0:3], v[80:83], v[40:43], v[0:3]\n"
      "ds_read_b64_tr_b16 v[80:81], v143 offset:25600\n"
      "ds_read_b64_tr_b16 v[82:83], v143 offset:32000\n"
      "v_mfma_f32_16x16x32_bf16 v[4:7], v[84:87], v[40:43], v[4:7]\n"
      "ds_read_b64_tr_b16 v[84:85], v143 offset:25632\n"
      "ds_read_b64_tr_b16 v[86:87], v143 offset:32032\n"
      "v_mfma_f32_16x16x32_bf16 v[8:11], v[88:91], v[40:43], v[8:11]\n"
      "ds_read_b64_tr_b16 v[88:89], v143 offset:25664\n"
      "ds_read_b64_tr_b16 v[90:91], v143 offset:32064\n"
      "v_mfma_f32_16x16x32_bf16 v[12:15], v[92:95], v[40:43], v[12:15]\n"
      "ds_read_b64_tr_b16 v[92:93], v143 offset:25696\n"
      "ds_read_b64_tr_b16 v[94:95], v143 offset:32096\n"
      "v_mfma_f32_16x16x32_bf16 v[16:19], v[96:99], v[40:43], v[16:19]\n"
      "ds_read_b64_tr_b16 v[96:97], v143 offset:25728\n"
      "ds_read_b64_tr_b16 v[98:99], v143 offset:32128\n"
      "s_setprio 1\n"
      "s_getpc_b64 %[ret]\n"
      "s_waitcnt vmcnt(5)\n"
      "v_max3_u32 v140, v44, v45, v46\n"
      "v_max_u32 v140, v140, v47\n"
      "v_cmp_eq_u32 vcc, -1, v140\n"
      "s_cbranch_vccnz 56f\n"
      "s_waitcnt lgkmcnt(15)\n"
      "v_mfma_f32_16x16x32_bf16 v[0:3], v[100:103], v[44:47], v[0:3]\n"
      "v_mfma_f32_16x16x32_bf16 v[4:7], v[104:107], v[44:47], v[4:7]\n"
      "v_mfma_f32_16x16x32_bf16 v[8:11], v[108:111], v[44:47], v[8:11]\n"
      "v_mfma_f32_16x16x32_bf16 v[12:15], v[112:115], v[44:47], v[12:15]\n"
      "v_mfma_f32_16x16x32_bf16 v[16:19], v[116:119], v[44:47], v[16:19]\n"
      "s_getpc_b64 %[ret]\n"
      "s_waitcnt vmcnt(4)\n"
      "v_max3_u32 v140, v48, v49, v50\n"
      "v_max_u32 v140, v140, v51\n"
      "v_cmp_eq_u32 vcc, -1, v140\n"
      "s_cbranch_vccnz 57f\n"
      "s_waitcnt lgkmcnt(15)\n"
      "v_mfma_f32_16x16x32_bf16 v[0:3], v[120:123], v[48:51], v[0:3]\n"
      "v_mfma_f32_16x16x32_bf16 v[4:7], v[124:127], v[48:51], v[4:7]\n"
      "v_mfma_f32_16x16x32_bf16 v[8:11], v[128:131], v[48:51], v[8:11]\n"
      "v_mfma_f32_16x16x32_bf16 v[12:15], v[132:135], v[48:51], v[12:15]\n"
      "v_mfma_f32_16x16x32_bf16 v[16:19], v[136:139], v[48:51], v[16:19]\n"
      "s_setprio 0\n"
      "s_getpc_b64 %[ret]\n"
      "s_waitcnt vmcnt(2)\n"
      "v_max3_u32 v140, v52, v53, v54\n"
      "v_max_u32 v140, v140, v55\n"
      "v_cmp_eq_u32 vcc, -1, v140\n"
      "s_cbranch_vccnz 58f\n"
      "s_waitcnt lgkmcnt(10)\n"
      "v_mfma_f32_16x16x32_bf16 v[0:3], v[60:63], v[52:55], v[0:3]\n"
      "v_mfma_f32_16x16x32_bf16 v[4:7], v[64:67], v[52:55], v[4:7]\n"
      "v_mfma_f32_16x16x32_bf16 v[8:11], v[68:71], v[52:55], v[8:11]\n"
      "v_mfma_f32_16x16x32_bf16 v[12:15], v[72:75], v[52:55], v[12:15]\n"
      "v_mfma_f32_16x16x32_bf16 v[16:19], v[76:79], v[52:55], v[16:19]\n"
      "s_getpc_b64 %[ret]\n"
      "s_waitcnt vmcnt(0)\n"
      "v_max3_u32 v140, v56, v57, v58\n"
      "v_max_u32 v140, v140, v59\n"
      "v_cmp_eq_u32 vcc, -1, v140\n"
      "s_cbranch_vccnz 59f\n"
      "s_waitcnt lgkmcnt(0)\n"
      "v_mfma_f32_16x16x32_bf16 v[0:3], v[80:83], v[56:59], v[0:3]\n"
      "v_mfma_f32_16x16x32_bf16 v[4:7], v[84:87], v[56:59], v[4:7]\n"
      "v_mfma_f32_16x16x32_bf16 v[8:11], v[88:91], v[56:59], v[8:11]\n"
      "v_mfma_f32_16x16x32_bf16 v[12:15], v[92:95], v[56:59], v[12:15]\n"
      "v_mfma_f32_16x16x32_bf16 v[16:19], v[96:99], v[56:59], v[16:19]\n"
      "s_branch 7f\n"
      "s_waitcnt vmcnt(11)\n"
      "v_max3_u32 v140, v20, v21, v22\n"
      "v_max_u32 v140, v140, v23\n"
      "v_cmp_eq_u32 vcc, -1, v140\n"
      "s_cbranch_vccz 6f\n"
      "50:\n"
      "global_load_dwordx4 v[20:23], v141, %[ob0] offset:0 sc1\n"
      "6:\n"
      "s_waitcnt vmcnt(10)\n"
      "v_max3_u32 v140, v24, v25, v26\n"
      "v_max_u32 v140, v140, v27\n"
      "v_cmp_eq_u32 vcc, -1, v140\n"
      "s_cbranch_vccz 6f\n"
      "51:\n"
      "global_load_dwordx4 v[24:27], v141, %[ob0] offset:1024 sc1\n"
      "6:\n"
      "s_waitcnt vmcnt(9)\n"
      "v_max3_u32 v140, v28, v29, v30\n"
      "v_max_u32 v140, v140, v31\n"
      "v_cmp_eq_u32 vcc, -1, v140\n"
      "s_cbranch_vccz 6f\n"
      "52:\n"
      "global_load_dwordx4 v[28:31], v141, %[ob0] offset:2048 sc1\n"
      "6:\n"
      "s_waitcnt vmcnt(8)\n"
      "v_max3_u32 v140, v32, v33, v34\n"
      "v_max_u32 v140, v140, v35\n"
      "v_cmp_eq_u32 vcc, -1, v140\n"
      "s_cbranch_vccz 6f\n"
      "53:\n"
      "global_load_dwordx4 v[32:35], v141, %[ob0] offset:3072 sc1\n"
      "6:\n"
      "s_waitcnt vmcnt(7)\n"
      "v_max3_u32 v140, v36, v37, v38\n"
      "v_max_u32 v140, v140, v39\n"
      "v_cmp_eq_u32 vcc, -1, v140\n"
      "s_cbranch_vccz 6f\n"
      "54:\n"
      "global_load_dwordx4 v[36:39], v141, %[ob1] offset:0 sc1\n"
      "6:\n"
      "s_waitcnt vmcnt(6)\n"
      "v_max3_u32 v140, v40, v41, v42\n"
      "v_max_u32 v140, v140, v43\n"
      "v_cmp_eq_u32 vcc, -1, v140\n"
      "s_cbranch_vccz 6f\n"
      "55:\n"
      "global_load_dwordx4 v[40:43], v141, %[ob1] offset:1024 sc1\n"
      "6:\n"
      "s_waitcnt vmcnt(5)\n"
      "v_max3_u32 v140, v44, v45, v46\n"
      "v_max_u32 v140, v140, v47\n"
      "v_cmp_eq_u32 vcc, -1, v140\n"
      "s_cbranch_vccz 6f\n"
      "56:\n"
      "global_load_dwordx4 v[44:47], v141, %[ob1] offset:2048 sc1\n"
      "6:\n"
      "s_waitcnt vmcnt(4)\n"
      "v_max3_u32 v140, v48, v49, v50\n"
      "v_max_u32 v140, v140, v51\n"
      "v_cmp_eq_u32 vcc, -1, v140\n"
      "s_cbranch_vccz 6f\n"
      "57:\n"
      "global_load_dwordx4 v[48:51], v141, %[ob1] offset:3072 sc1\n"
      "6:\n"
      "s_waitcnt vmcnt(2)\n"
      "v_max3_u32 v140, v52, v53, v54\n"
      "v_max_u32 v140, v140, v55\n"
      "v_cmp_eq_u32 vcc, -1, v140\n"
      "s_cbranch_vccz 6f\n"
      "58:\n"
      "global_load_dwordx2 v[52:53], %[arem], %[ob2] offset:0 sc1\n"
      "global_load_dwordx2 v[54:55], %[arem], %[ob2] offset:128 sc1\n"
      "6:\n"
      "s_waitcnt vmcnt(0)\n"
      "v_max3_u32 v140, v56, v57, v58\n"
      "v_max_u32 v140, v140, v59\n"
      "v_cmp_eq_u32 vcc, -1, v140\n"
      "s_cbranch_vccz 6f\n"
      "59:\n"
      "global_load_dwordx2 v[56:57], %[arem], %[ob2] offset:1024 sc1\n"
      "global_load_dwordx2 v[58:59], %[arem], %[ob2] offset:1152 sc1\n"
      "6:\n"
      "s_add_u32 %[spin], %[spin], 1\n"
      "s_cmp_gt_u32 %[spin], %[cap]\n"
      "s_cbranch_scc1 7f\n"
      "s_waitcnt vmcnt(0)\n"
      "s_setpc_b64 %[ret]\n"
      "7:\n"
      "s_waitcnt vmcnt(0) lgkmcnt(0)\n"
      "s_nop 7\n"
        : "=&{v[0:3]}"(acc[0]), "=&{v[4:7]}"(acc[1]), "=&{v[8:11]}"(acc[2]), "=&{v[12:15]}"(acc[3]), "=&{v[16:19]}"(acc[4]), [spin] "=&s"(spin), [ret] "=&s"(ret),

          "+v"(exw)
        : "{v141}"(l16), "{v142}"(am), "{v143}"(ar), [ob0] "s"(osrc), [ob1] "s"(osrc + 4096), [ob2] "s"(osrc + 8192), [cap] "s"(SPIN_CAP),
          [arem] "v"(arem)
        : "v20", "v21", "v22", "v23", "v24", "v25", "v26", "v27", "v28", "v29", "v30", "v31", "v32", "v33", "v34", "v35", "v36", "v37", "v38", "v39", "v40", "v41", "v42", "v43", "v44", "v45", "v46", "v47", "v48", "v49", "v50", "v51", "v52", "v53", "v54", "v55", "v56", "v57", "v58", "v59", "v60", "v61", "v62", "v63", "v64", "v65", "v66", "v67", "v68", "v69", "v70", "v71", "v72", "v73", "v74", "v75", "v76", "v77", "v78", "v79", "v80", "v81", "v82", "v83", "v84", "v85", "v86", "v87", "v88", "v89", "v90", "v91", "v92", "v93", "v94", "v95", "v96", "v97", "v98", "v99", "v100", "v101", "v102", "v103", "v104", "v105", "v106", "v107", "v108", "v109", "v110", "v111", "v112", "v113", "v114", "v115", "v116", "v117", "v118", "v119", "v120", "v121", "v122", "v123", "v124", "v125", "v126", "v127", "v128", "v129", "v130", "v131", "v132", "v133", "v134", "v135", "v136", "v137", "v138", "v139", "v140", "vcc", "scc", "memory");

    }

#pragma unroll
    for (int t = 0; t < 5; ++t) *(LAS f32x4*)(size_t)(exw + t * 64) = acc[t];
    u32x4 xr[2]; float gt[2];
#pragma unroll
    for (int n = 0; n < 2; ++n) { xr[n] = *(LAS const u32x4*)(size_t)(L + L_FD + ((n && w < 2 ? 512 : 0) + w * 64) * 16 + l16); gt[n] = *(LAS const float*)(size_t)gad[n]; }
    BAR();
#pragma unroll
    for (int n = 0; n < 2; ++n) {
      if (n && w >= 2) break;
      const f32x4 a0 = *(LAS const f32x4*)(size_t)(exr[n]) + *(LAS const f32x4*)(size_t)(exr[n] + EXH);
      const f32x4 a1 = *(LAS const f32x4*)(size_t)(exr[n] + 16) + *(LAS const f32x4*)(size_t)(exr[n] + EXH + 16);
      const float ga = gt[n], gb = rbf(1.0f - ga);
      const u32x4 v = {pk2(a0[0] * ga + bflo(xr[n][0]) * gb, a0[1] * ga + bfhi(xr[n][0]) * gb), pk2(a0[2] * ga + bflo(xr[n][1]) * gb, a0[3] * ga + bfhi(xr[n][1]) * gb),
                       pk2(a1[0] * ga + bflo(xr[n][2]) * gb, a1[1] * ga + bfhi(xr[n][2]) * gb), pk2(a1[2] * ga + bflo(xr[n][3]) * gb, a1[3] * ga + bfhi(xr[n][3]) * gb)};
      st128_dev(out, outo[n], v);
    }

  }

}

extern "C" void kernel_launch(void* const* d_in, const int* in_sizes, int n_in, void* d_out, int out_size, void* d_ws, size_t ws_size,
                              hipStream_t stream) {
  (void)hipFuncSetAttribute((const void*)k_fused, hipFuncAttributeMaxDynamicSharedMemorySize, LDS_BYTES);
  k_fused<<<256, 768, LDS_BYTES, stream>>>((const u16*)d_in[0], (const u16*)d_in[1], (const u16*)d_in[2], (const u16*)d_in[3], (const u16*)d_in[4],
                                           (const u16*)d_in[6], (const u16*)d_in[7], (const u16*)d_in[8], (const u16*)d_in[9], (const u16*)d_in[10],
                                           (char*)d_ws, (u16*)d_out);
}
```
